```python
import torch

def _softmax(x, axis=-1):
    return torch.softmax(x, dim=axis)

def _gelu(x):
    return torch.nn.functional.gelu(x, approximate='tanh')

def _silu(x):
    return torch.nn.functional.silu(x)

def _sigmoid(x):
    return torch.sigmoid(x)

def _layernorm(x, axis=-1, eps=1e-05):
    m = x.mean(dim=axis, keepdim=True)
    v = x.var(dim=axis, keepdim=True, unbiased=False)
    return (x - m) / torch.sqrt(v + eps)

def _scatter_add(tgt, idx, src, axis=0):
    return tgt.clone().index_add_(axis, idx, src)

def _segment_sum(src, seg, n):
    out = torch.zeros((n,) + src.shape[1:], dtype=src.dtype)
    return out.index_add_(0, seg, src)

def _pad(x, pw):
    flat = tuple((v for pair in reversed(pw) for v in pair))
    return torch.nn.functional.pad(x, flat)
kernel_name = 'gated_mix__memory_aug_recurrent_mem_tr__mixer_channel_mlp__prompt'

def setup_inputs():

    _w = lambda a: (lambda t: t.to(torch.bfloat16) if t.is_floating_point() else t)(torch.as_tensor(a))

    def _A_setup():
        B, S, Dm = (1, 2048, 448)
        H, Dh = (16, 256)
        M, seg = (8, 32)
        seg = min(seg, S)
        S = S // seg * seg
        return {'x': _w((lambda *s: torch.randn(*s, dtype=torch.bfloat16) * 0.05)(B, S, Dm)), 'mem0': _w((lambda *s: torch.randn(*s, dtype=torch.bfloat16) * 0.8)(B, M, Dm)), 'Wq': _w((lambda *s: torch.randn(*s, dtype=torch.bfloat16) * 1.6)(Dm, H, Dh) * Dm ** (-0.5)), 'Wk': _w((lambda *s: torch.randn(*s, dtype=torch.bfloat16) * 1.6)(Dm, H, Dh) * Dm ** (-0.5)), 'Wv': _w((lambda *s: torch.randn(*s, dtype=torch.bfloat16) * 0.8)(Dm, H, Dh) * Dm ** (-0.5)), 'Wo': _w((lambda *s: torch.randn(*s, dtype=torch.bfloat16) * 0.8)(H, Dh, Dm) * (H * Dh) ** (-0.5)), 'seg_len': seg}
    _A_in = _A_setup()

    def _B_setup():
        B, S, D, F = (1, 2048, 448, 1792)
        return {'x': _w((lambda *s: torch.randn(*s, dtype=torch.bfloat16) * 0.05)(B, S, D)), 'w1': _w((lambda *s: torch.randn(*s, dtype=torch.bfloat16) * 1.6)(D, F) * D ** (-0.5)), 'b1': _w((lambda *s: torch.randn(*s, dtype=torch.bfloat16) * 0.4)(F)), 'w2': _w((lambda *s: torch.randn(*s, dtype=torch.bfloat16) * 0.05)(F, D) * F ** (-0.5)), 'b2': _w((lambda *s: torch.randn(*s, dtype=torch.bfloat16) * 0.05)(D))}
    _B_in = _B_setup()
    return {'A_x': _A_in['x'], 'A_mem0': _A_in['mem0'], 'A_Wq': _A_in['Wq'], 'A_Wk': _A_in['Wk'], 'A_Wv': _A_in['Wv'], 'A_Wo': _A_in['Wo'], 'A_seg_len': _A_in['seg_len'], 'B_x': _B_in['x'], 'B_w1': _B_in['w1'], 'B_b1': _B_in['b1'], 'B_w2': _B_in['w2'], 'B_b2': _B_in['b2'], 'gate_w': _w((lambda *s: torch.randn(*s, dtype=torch.bfloat16) * 0.05)(448) * 16.0)}

def reference(A_x, A_mem0, A_Wq, A_Wk, A_Wv, A_Wo, A_seg_len, B_x, B_w1, B_b1, B_w2, B_b2, gate_w):

    def _A_ref(x, mem0, Wq, Wk, Wv, Wo, seg_len):
        B, S, Dm = x.shape
        H, Dh = (Wq.shape[1], Wq.shape[2])
        M = mem0.shape[1]
        scale = Dh ** (-0.5)
        n_seg = S // seg_len
        L = M + seg_len
        idx = torch.arange(L)
        mask = (idx[:, None] < M) | (idx[:, None] >= idx[None, :])
        mem = mem0
        outs = []
        for s in range(n_seg):
            xs = x[:, s * seg_len:(s + 1) * seg_len, :].clone()
            h = torch.concatenate([mem, xs], axis=1)
            q = torch.einsum('btd,dhe->bhte', h, Wq)
            k = torch.einsum('btd,dhe->bhte', h, Wk)
            v = torch.einsum('btd,dhe->bhte', h, Wv)
            scores = torch.einsum('bhqe,bhke->bhqk', q, k) * scale
            scores = torch.where(mask[None, None], scores, -1000000000.0)
            p = _softmax(scores, axis=-1)
            o = torch.einsum('bhqk,bhke->bhqe', p, v)
            o = torch.einsum('bhte,hed->btd', o, Wo)
            mem = o[:, :M, :].clone()
            outs.append(o[:, M:, :])
        return torch.concatenate(outs, axis=1)

    def _B_ref(x, w1, b1, w2, b2):
        y = _layernorm(x, axis=-1)
        h = _gelu(torch.einsum('bsd,df->bsf', y, w1) + b1)
        return torch.einsum('bsf,fd->bsd', h, w2) + b2
    x = A_x
    g = _sigmoid((x * gate_w).sum(-1, keepdims=True))
    a = _A_ref(x, A_mem0, A_Wq, A_Wk, A_Wv, A_Wo, A_seg_len)
    b = _B_ref(x, B_w1, B_b1, B_w2, B_b2)
    return g * a + (1 - g) * b
```

```cpp
#include <hip/hip_runtime.h>
#include <hip/hip_bf16.h>
typedef unsigned u32; typedef unsigned long long u64; typedef unsigned short u16;
typedef u32 u32x2 __attribute__((ext_vector_type(2)));
typedef u32 u32x4 __attribute__((ext_vector_type(4)));
typedef u32 u32x8 __attribute__((ext_vector_type(8)));
typedef float f32x2 __attribute__((ext_vector_type(2)));
typedef float f32x4 __attribute__((ext_vector_type(4)));
typedef __bf16 bf16x2 __attribute__((ext_vector_type(2)));
typedef __bf16 bf16x8 __attribute__((ext_vector_type(8)));
typedef __bf16 bf16x16 __attribute__((ext_vector_type(16)));
typedef float f32x16 __attribute__((ext_vector_type(16)));
typedef short s16x4 __attribute__((ext_vector_type(4)));
typedef short s16x8 __attribute__((ext_vector_type(8)));
#define DEV __device__ __forceinline__
template<class T,class U> __device__ __forceinline__ T bcf(U x){ return __builtin_bit_cast(T,x); }
#define BC(T,x) bcf<T>(x)

constexpr int D=448, H=16, DH=256, S=2048, NSEG=64, F=1792, J=2, NV=28;
constexpr float CSC = 1.4426950408889634f/16.0f;
constexpr size_t O_HOT=0;
constexpr size_t O_XOR=O_HOT+(size_t)66*65536;
constexpr size_t O_FLG=O_XOR+32768;
constexpr size_t O_A  =O_XOR+65536;
constexpr size_t O_BT =O_A+(size_t)H*D*D*2;
constexpr size_t O_KQ =O_BT+(size_t)H*D*D*2;
constexpr size_t O_VO =O_KQ+(size_t)NSEG*H*14*2048;
constexpr size_t O_UP =O_VO+(size_t)NSEG*NV*8*2048;
constexpr size_t O_SXM=O_UP+(size_t)NSEG*NV*8*512;
constexpr size_t O_SXX=O_SXM+(size_t)NSEG*H*J*1024;
constexpr size_t O_BB =O_SXX+(size_t)NSEG*H*J*4096;
constexpr size_t O_G  =O_BB+(size_t)S*D*4;
constexpr size_t O_W1 =O_G+(size_t)S*4;
constexpr size_t O_W2 =O_W1+(size_t)D*F*2;
constexpr size_t O_END=O_W2+(size_t)D*F*2;

DEV float bf2f(u16 v){ return BC(float,(u32)v<<16); }
DEV u16 f2bf(float f){ __bf16 b=(__bf16)f; return BC(u16,b); }
DEV u32 pk(float a,float b){ bf16x2 v={(__bf16)a,(__bf16)b}; return BC(u32,v); }
DEV __amdgpu_buffer_rsrc_t mk(const void* p, u32 bytes){ return __builtin_amdgcn_make_buffer_rsrc((void*)p,0,bytes,0x00020000); }
DEV u32 now(){ u64 t; asm volatile("s_memtime %0\n s_waitcnt lgkmcnt(0)":"=s"(t)::"memory"); return (u32)t; }
DEV f32x4 smf64(u32x4 a,u32x8 b,f32x4 c,int idx){ return __builtin_amdgcn_smfmac_f32_16x16x64_bf16(BC(bf16x8,a),BC(bf16x16,b),c,idx,0,0); }
DEV f32x4 smf32(u32x2 a,u32x4 b,f32x4 c,int idx){ return __builtin_amdgcn_smfmac_f32_16x16x32_bf16(BC(s16x4,a),BC(s16x8,b),c,idx,0,0); }
DEV float swadd(float a,float b){ auto r=__builtin_amdgcn_permlane32_swap(BC(u32,a),BC(u32,b),false,false); return BC(float,r[0])+BC(float,r[1]); }
template<int C> DEV float dpp(float x){ return BC(float,__builtin_amdgcn_update_dpp(0,BC(int,x),C,0xF,0xF,true)); }
DEV float vmax3(float a,float b,float c){ float r; asm("v_max3_f32 %0,%1,%2,%3":"=v"(r):"v"(a),"v"(b),"v"(c)); return r; }
template<int C> DEV float maxdpp(float x){ float y=dpp<C>(x); return vmax3(x,y,y); }
DEV u32x8 cat(u32x4 a,u32x4 b){ return __builtin_shufflevector(a,b,0,1,2,3,4,5,6,7); }
DEV u32 canon(u32 x){ return x>0xFFFFFFFEu?0xFFFFFFFEu:x; }
DEV u32x2 trread(const char* p){ return BC(u32x2,__builtin_amdgcn_ds_read_tr16_b64_v4i16((__attribute__((address_space(3))) s16x4*)p)); }
constexpr int SPIN_MAX=400000;
#define SENT 0xFFFFFFFFu
DEV u32 umax3(u32 a,u32 b,u32 c){ return max(max(a,b),c); }
DEV bool anysent(u32 m){ return __builtin_amdgcn_ballot_w64(m==SENT)!=0; }
DEV void nap(int n){ for(int i=0;i<n;i++) __builtin_amdgcn_s_sleep(1); }
DEV u32x4 poll16(__amdgpu_buffer_rsrc_t r,u32 off,int& budget){
  u32x4 v;
  #pragma unroll 1
  do { asm volatile("":::"memory"); v=__builtin_amdgcn_raw_buffer_load_b128(r,off,0,16);
  } while(anysent(max(umax3(v.x,v.y,v.z),v.w)) && --budget>0);
  return v;
}
DEV u32 loadX(const char* ws){ const u32* xr=(const u32*)(ws+O_XOR); u32 X=0; for(int i=0;i<9;i++) X|=(xr[i]>>13)<<(2*i); return __builtin_amdgcn_readfirstlane(X); }
#define XM(s) (((XX>>(((s)>>3)*2))&3u)<<13)

DEV f32x16 mf32(u32x4 a,u32x4 b,f32x16 c){ return __builtin_amdgcn_mfma_f32_32x32x16_bf16(BC(bf16x8,a),BC(bf16x8,b),c,0,0,0); }
DEV f32x4 mf16(u32x4 a,u32x4 b,f32x4 c){ return __builtin_amdgcn_mfma_f32_16x16x32_bf16(BC(bf16x8,a),BC(bf16x8,b),c,0,0,0); }
DEV f32x4 mf16k(u32x2 a,u32x2 b,f32x4 c){ return __builtin_amdgcn_mfma_f32_16x16x16bf16_1k(BC(s16x4,a),BC(s16x4,b),c,0,0,0); }
DEV int sw23(int m){ return (m&19)|((m&4)<<1)|((m&8)>>1); }
DEV u32 wf(int h,int row,int kc){ return (u32)(((h*14+(row>>5))*56+kc)*512+(row&31)*16); }
DEV void vmwait(){ asm volatile("s_waitcnt vmcnt(0)":::"memory"); }
DEV float lo16(u32 v){ return BC(float,v<<16); }
DEV float hi16(u32 v){ return BC(float,v&0xFFFF0000u); }
DEV int waitprog(__amdgpu_buffer_rsrc_t fl,int idx,int need,int& budget,int slp){
  int v;
  #pragma unroll 1
  while(true){ asm volatile("":::"memory"); v=(int)__builtin_amdgcn_raw_buffer_load_b32(fl,idx*128,0,16);
    if(__builtin_amdgcn_ballot_w64(v<need)==0 || --budget<=0) break; nap(slp); }
  return v;
}

struct ChainArgs{ const u16* x; char* ws; const u16* b1; const u16* b2; const u16* gw; u16* out; int dA; int dB; int dC; int dD; int ts; int pad; };
constexpr int L_MEM=0, L_TT=7168, L_SX=L_TT+2*2112+64, L_L=L_SX+1024, L_RED=7168, L_U=L_RED+4096;


#define LD128(r,o,aux) __builtin_amdgcn_raw_buffer_load_b128(r,o,0,aux)
#define LD64(r,o,aux) __builtin_amdgcn_raw_buffer_load_b64(r,o,0,aux)
#define ST64(v,r,o,aux) __builtin_amdgcn_raw_buffer_store_b64(v,r,o,0,aux)
#define ST32(v,r,o,aux) __builtin_amdgcn_raw_buffer_store_b32(v,r,o,0,aux)

template<int ROLE>
DEV void score_loop(const ChainArgs& P,char* lds,int slot,int w,int l){
  const int r=l&15,g=l>>4; char* ws=P.ws;
  const int h=slot>>1, j=slot&1, n0=224*j;
  auto hot=mk(ws+O_HOT,66*65536);
  const u32 XX=loadX(ws);
  const int idx=(r<8)?0x4444:0xEEEE;
  int dead=SPIN_MAX;
  u32x8 W[2][7];
  auto kq=mk(ws+O_KQ,NSEG*H*14*2048); auto flg=mk(ws+O_FLG,16384); int have=0;
  if(ROLE<3){
    #pragma unroll
    for(int tt=0;tt<2;tt++){ const char* p=ws+O_A+wf(h,n0+32*w+16*tt+r,g);
      #pragma unroll
      for(int t=0;t<7;t++) W[tt][t]=cat(*(const u32x4*)(p+4096*t),*(const u32x4*)(p+4096*t+2048)); }
  } else if(ROLE==3){
    { int v=waitprog(flg,2*h+(l&1),1,dead,1); have=min(__builtin_amdgcn_readlane(v,0),__builtin_amdgcn_readlane(v,1)); }
    #pragma unroll
    for(int k=0;k<14;k++){ u32 o=(u32)h*14*2048+k*2048+l*16; W[k/7][k%7]=cat(LD128(kq,o,16),LD128(kq,o+1024,16)); }
  }
  if(ROLE==0){ *(u32x2*)(lds+L_TT+(l>>5)*2112+(224+(l&31))*8)=u32x2{0,0}; if(j==1&&l<8) *(float*)(lds+L_L+l*4)=0.f; }
  if(ROLE==3) __builtin_amdgcn_s_setprio(3); if(ROLE==0) __builtin_amdgcn_s_setprio(2);
  u32x4 xb[7];
  auto xrs=mk(P.x,S*D*2);
  const u32 xo=(u32)((16*(w-1)+r)*D+n0+8*g)*2;
  if(ROLE==1){
    #pragma unroll
    for(int k=0;k<7;k++) xb[k]=LD128(xrs,xo+64*k,0);
  }
  const int rr=r>>2, hp=(l>>1)&1, q1=l&1;
  const char* trp=lds+L_TT+q1*2112+(32*(g>>1)+16*(g&1)+(ROLE==0?4*hp+rr:2*hp+(rr&1)+4*(rr>>1)))*8;
  auto sxm=mk(ws+O_SXM,NSEG*H*J*1024);

  for(int s=0;s<NSEG;s++){
    const u32 X=XM(s); const u32 base=s*65536u;
    if(ROLE<3){ nap(ROLE==0?P.dA:P.dD); u32x4 v=poll16(hot,base+((w*1024+l*16)^X),dead);  *(u32x4*)(lds+L_MEM+w*1024+l*16)=v; }
    __syncthreads();
    if(ROLE==4){  } else {  }
    float v0,v1,v2,v3,xrl; u32x2 mb[14]; u32x4 am[7];
    if(ROLE<4){
      f32x4 c0={0,0,0,0},c1={0,0,0,0};
      #pragma unroll
      for(int t=0;t<7;t++) am[t]=*(const u32x4*)(lds+L_MEM+t*1024+l*16);
      __builtin_amdgcn_sched_barrier(0);
      #pragma unroll
      for(int t=0;t<7;t++){ c0=smf64(am[t],W[0][t],c0,idx); c1=smf64(am[t],W[1][t],c1,idx); }
      if(ROLE==0){
        #pragma unroll
        for(int k=0;k<7;k++){ const char* p=lds+L_MEM+(14*j+2*k+(g>>1))*256+(l&7)*16+(g&1)*8; mb[2*k]=*(const u32x2*)p; mb[2*k+1]=*(const u32x2*)(p+128); }
      }
      v0=swadd(c0[0],c1[0]);v1=swadd(c0[1],c1[1]);v2=swadd(c0[2],c1[2]);v3=swadd(c0[3],c1[3]);
      asm volatile("":"+v"(v0),"+v"(v3));
      if(ROLE<3) *(u32x2*)(lds+L_TT+(g&1)*2112+(32*w+16*(l>>5)+r)*8)=u32x2{pk(v0,v1),pk(v2,v3)};
      if(ROLE==3){
        float* sx=(float*)(lds+L_SX)+(4*(g&1))*32+16*(l>>5)+r;
        sx[0]=v0; sx[32]=v1; sx[64]=v2; sx[96]=v3;
        __builtin_amdgcn_wave_barrier();
        f32x4 z=*(const f32x4*)(lds+L_SX+l*16);
        float m=vmax3(vmax3(z[0],z[1],z[2]),z[3],z[3]);
        m=maxdpp<0xB1>(m); m=maxdpp<0x4E>(m); m=maxdpp<0x141>(m);
        float mc=m*CSC;
        v0=__builtin_amdgcn_exp2f(z[0]*CSC-mc);v1=__builtin_amdgcn_exp2f(z[1]*CSC-mc);v2=__builtin_amdgcn_exp2f(z[2]*CSC-mc);v3=__builtin_amdgcn_exp2f(z[3]*CSC-mc);
        float sm=(v0+v1)+(v2+v3);
        sm+=dpp<0xB1>(sm); sm+=dpp<0x4E>(sm); sm+=dpp<0x141>(sm);
        *(float*)(lds+L_L+(l>>3)*4)=mc+__builtin_amdgcn_logf(sm);
        xrl=sm;
      }
    }
    __syncthreads();
    if(ROLE<4){  }
    if(ROLE==0){
      if(s<NSEG-1){
        f32x4 c={0,0,0,0}; u32x2 ta[8];
        #pragma unroll
        for(int u=0;u<8;u++) ta[u]=trread(trp+(u>>1)*512+(u&1)*64);
        const float L=*(const float*)(lds+L_L+(l&7)*4);
        __builtin_amdgcn_sched_barrier(0);
        #pragma unroll
        for(int u=0;u<4;u++){
          u32x2 a0=ta[2*u], a1=ta[2*u+1];
          u32x4 f0={mb[4*u].x,mb[4*u].y,mb[4*u+1].x,mb[4*u+1].y};
          u32x4 f1= u<3? u32x4{mb[(4*u+2)%14].x,mb[(4*u+2)%14].y,mb[(4*u+3)%14].x,mb[(4*u+3)%14].y} : u32x4{0,0,0,0};
          c=smf64(u32x4{a0.x,a0.y,a1.x,a1.y},cat(f0,f1),c,idx);
        }
        float sa=swadd(c[0],c[2])*CSC-L, sb=swadd(c[1],c[3])*CSC-L;
        if(r<8) ST64((u32x2{canon(BC(u32,sa)),canon(BC(u32,sb))}),hot,base+32768+(((h*J+j)*256+(g>>1)*128+r*16+(g&1)*8)^X),16);
      }

    } else if(ROLE==1){
      f32x4 c={0,0,0,0};
      #pragma unroll
      for(int u=0;u<4;u++){
        u32x2 a0=trread(trp+u*512), a1=trread(trp+u*512+64);
        c=smf64(u32x4{a0.x,a0.y,a1.x,a1.y},cat(xb[2*u],u<3?xb[(2*u+1)%7]:u32x4{0,0,0,0}),c,idx);
      }
      float sa=swadd(c[0],c[2]), sb=swadd(c[1],c[3]);
      ST64((u32x2{canon(BC(u32,sa)),canon(BC(u32,sb))}),sxm,((s*H+h)*J+j)*1024+(w-1)*512+(g&1)*256+r*16+(g>>1)*8,16);
      const u32 xn=xo+min(s+1,NSEG-1)*32*D*2;
      #pragma unroll
      for(int k=0;k<7;k++) xb[k]=LD128(xrs,xn+64*k,0);
    } else if(ROLE==3){
      float rl=__builtin_amdgcn_rcpf(xrl);
      ST64((u32x2{canon(pk(v0*rl,v1*rl)),canon(pk(v2*rl,v3*rl))}),hot,base+((16384+h*512+l*8)^X),16);

      if(have<min(s+2,NSEG)){ int v=waitprog(flg,2*h+(l&1),s+2,dead,1); have=min(__builtin_amdgcn_readlane(v,0),__builtin_amdgcn_readlane(v,1)); }
      const u32 o=(u32)(min(s+1,NSEG-1)*H+h)*14*2048+l*16;
      #pragma unroll
      for(int k=0;k<14;k++) W[k/7][k%7]=cat(LD128(kq,o+k*2048,16),LD128(kq,o+k*2048+1024,16));
    }
  }

}

template<int FIN>
DEV void value_loop(const ChainArgs& P,char* lds,int c,int w,int l){
  const int r=l&15,g=l>>4; char* ws=P.ws;
  auto hot=mk(ws+O_HOT,66*65536);
  const u32 XX=loadX(ws);
  const int idx=(r<8)?0x4444:0xEEEE;
  int dead=SPIN_MAX;
  const int pr=FIN?2*w:2*(w-4)+1;
  u32x8 W[2][7];
  #pragma unroll
  for(int hh=0;hh<2;hh++){ const char* p=ws+O_BT+wf(2*pr+hh,16*c+r,g);
    #pragma unroll
    for(int t=0;t<7;t++) W[hh][t]=cat(*(const u32x4*)(p+4096*t),*(const u32x4*)(p+4096*t+2048)); }
  auto flg=mk(ws+O_FLG,16384); int have=0;
  auto vor=mk(ws+O_VO,NSEG*NV*8*2048);
  auto upr=mk(ws+O_UP,NSEG*NV*8*512);
  u32 voo=(c*8+pr)*2048+l*16;
  u32x8 vo0,vo1;
  if(FIN){ have=__builtin_amdgcn_readfirstlane(waitprog(flg,32+c,1,dead,1));
    vo0=cat(LD128(vor,voo,16),LD128(vor,voo+1024,16)); vo1=cat(LD128(vor,voo+2048,16),LD128(vor,voo+3072,16)); __builtin_amdgcn_s_setprio(2); }
  const int i=l&7, hi=(l>>3)&1;
  const u32 dm0=(i==2*hi?0xFFFFu:0u)|(i==2*hi+1?0xFFFF0000u:0u), dm1=(i==4+2*hi?0xFFFFu:0u)|(i==5+2*hi?0xFFFF0000u:0u);
  const u32 oEX=16384+(4*w+hi)*512+i*64+g*16, oSM=(4*w+g)*J*256+hi*128+i*16;
  float* redw=(float*)(lds+L_RED)+w*144+((r>>1)&1)*72+(4*(g&1)+2*(l>>5))*8+(r>>2)*2+(r&1);
  const char* redr=lds+L_RED+((l>>5)*72+(l&31)*2)*4;
  char* ux=lds+L_U+(w&3)*512+l*8;

  for(int s=0;s<NSEG;s++){
    const u32 X=XM(s), X1=XM(s+1); const u32 base=s*65536u;
    if(!FIN){ nap(P.dC); const u32 o0=base+(((w-4)*2048+l*16)^X), o1=base+((min((w-4)*2048+1024,6144)+l*16)^X); u32x4 v0,v1;
      #pragma unroll 1
      do{ asm volatile("":::"memory"); v0=LD128(hot,o0,16); v1=LD128(hot,o1,16);
      } while(anysent(max(umax3(umax3(v0.x,v0.y,v0.z),v0.w,v1.x),umax3(v1.y,v1.z,v1.w))) && --dead>0);

      *(u32x4*)(lds+L_MEM+(w-4)*2048+l*16)=v0; if(w<7) *(u32x4*)(lds+L_MEM+(w-4)*2048+1024+l*16)=v1; }
    __syncthreads();

    f32x4 c0={0,0,0,0},c1={0,0,0,0}; u32x4 am[7];
    #pragma unroll
    for(int t=0;t<7;t++) am[t]=*(const u32x4*)(lds+L_MEM+t*1024+l*16);
    __builtin_amdgcn_sched_barrier(0);
    #pragma unroll
    for(int t=0;t<7;t++){ c0=smf64(am[t],W[0][t],c0,idx); c1=smf64(am[t],W[1][t],c1,idx); }
    u32x2 u={canon(pk(swadd(c0[0],c1[0]),swadd(c0[1],c1[1]))), canon(pk(swadd(c0[2],c1[2]),swadd(c0[3],c1[3])))};
    if(!FIN || s==NSEG-1) ST64(u,upr,((s*NV+c)*8+pr)*512+l*8,16);
    if(s==NSEG-1) break;
    if(!FIN) *(u32x2*)ux=u;
    __syncthreads();

    if(FIN){
      u32x2 u2=*(const u32x2*)ux;
      nap(P.dB);
      u32x4 e0,e1,p0,p1;
      #pragma unroll 1
      do{ asm volatile("":::"memory");
        e0=LD128(hot,base+(oEX^X),16); e1=LD128(hot,base+((oEX+1024)^X),16);
        p0=LD128(hot,base+32768+(oSM^X),16); p1=LD128(hot,base+32768+((oSM+256)^X),16);
      } while(anysent(max(umax3(umax3(umax3(e0.x,e0.y,e0.z),e0.w,e1.x),umax3(e1.y,e1.z,e1.w),p0.x),umax3(umax3(p0.y,p0.z,p0.w),umax3(p1.x,p1.y,p1.z),p1.w))) && --dead>0);

      f32x4 cx0=smf64(e0,vo0,f32x4{0,0,0,0},idx), cx1=smf64(e1,vo1,f32x4{0,0,0,0},idx);
      f32x4 t=BC(f32x4,p0)+BC(f32x4,p1);
      float m=vmax3(vmax3(t[0],t[1],t[2]),t[3],0.f); m=maxdpp<0x128>(m);
      float a0=__builtin_amdgcn_exp2f(t[0]-m),a1=__builtin_amdgcn_exp2f(t[1]-m),a2=__builtin_amdgcn_exp2f(t[2]-m),a3=__builtin_amdgcn_exp2f(t[3]-m),f=__builtin_amdgcn_exp2f(-m);
      float sm=(a0+a1)+(a2+a3); sm+=dpp<0x128>(sm);
      float rl=__builtin_amdgcn_rcpf(sm+f), fl=f*rl; u32 ff=pk(fl,fl);
      u32x8 bz={u.x,u.y,pk(cx0[0],cx0[1]),pk(cx0[2],cx0[3]),u2.x,u2.y,pk(cx1[0],cx1[1]),pk(cx1[2],cx1[3])};
      f32x4 o=smf64(u32x4{pk(a0*rl,a1*rl),ff&dm0,pk(a2*rl,a3*rl),ff&dm1},bz,f32x4{0,0,0,0},idx);
      redw[0]=swadd(o[0],o[2]); redw[8]=swadd(o[1],o[3]);
    }
    __syncthreads();
    if(w==7){
      f32x2 a=(*(const f32x2*)redr+*(const f32x2*)(redr+576))+(*(const f32x2*)(redr+2*576)+*(const f32x2*)(redr+3*576));
      ST32(canon(pk(a[0],a[1])),hot,base+65536+((c*256+l*4)^X1),16);
    }

    if(FIN){ if(have<s+2) have=__builtin_amdgcn_readfirstlane(waitprog(flg,32+c,s+2,dead,1));
      voo+=NV*8*2048; vo0=cat(LD128(vor,voo,16),LD128(vor,voo+1024,16)); vo1=cat(LD128(vor,voo+2048,16),LD128(vor,voo+3072,16));
      ST64(u,upr,((s*NV+c)*8+pr)*512+l*8,16); }
  }

}

#define ST128(v,r,o,aux) __builtin_amdgcn_raw_buffer_store_b128(v,r,o,0,aux)
constexpr int XROW=912, XBUF=32*XROW;
struct XStage{ u32x4 v[4]; int la[4]; };
DEV void xs_init(XStage& X,int tid){
  #pragma unroll
  for(int i=0;i<4;i++){ int p=min(tid+512*i,1791); X.la[i]=(p/56)*XROW+(p%56)*16; } }
DEV void xs_load(XStage& X,__amdgpu_buffer_rsrc_t xrs,int s,int tid){
  #pragma unroll
  for(int i=0;i<4;i++) X.v[i]=LD128(xrs,s*28672+min(tid+512*i,1791)*16,0); }
DEV void xs_put(const XStage& X,char* buf,int tid){
  #pragma unroll
  for(int i=0;i<4;i++) if(i<3||tid<256) *(u32x4*)(buf+X.la[i])=X.v[i]; }
DEV u32x4 pk8(const f32x16& a,int o){ return u32x4{pk(a[o],a[o+1]),pk(a[o+2],a[o+3]),pk(a[o+4],a[o+5]),pk(a[o+6],a[o+7])}; }

template<int SX>
DEV void kq_worker(const ChainArgs& P,char* lds,int slot,int w,int l,int tid){
  char* ws=P.ws; const int h=slot>>1,j=slot&1,m=l&31,hh=l>>5;
  auto xrs=mk(P.x,S*D*2); auto kq=mk(ws+O_KQ,NSEG*H*14*2048); auto sxx=mk(ws+O_SXX,NSEG*H*J*4096); auto flg=mk(ws+O_FLG,16384);
  XStage X; xs_init(X,tid); xs_load(X,xrs,0,tid);
  u32x4 W[28];
  if(!SX){ const char* p=ws+O_A+wf(h,224*j+32*w+sw23(m),hh);
    #pragma unroll
    for(int t=0;t<28;t++) W[t]=*(const u32x4*)(p+1024*t); }
  xs_put(X,lds,tid); xs_load(X,xrs,1,tid);
  __syncthreads();
  char* kl=lds+3*XBUF;
  int b0=0,b1=XBUF,b2=2*XBUF;
  for(int s=0;s<=NSEG;s++){
    xs_put(X,lds+b1,tid); xs_load(X,xrs,min(s+2,NSEG-1),tid);
    f32x16 c;
    #pragma unroll
    for(int i=0;i<16;i++) c[i]=0.f;
    if(!SX){ if(s<NSEG){
      const char* xp=lds+b0+m*XROW+hh*16;
      #pragma unroll
      for(int t=0;t<28;t++) c=mf32(W[t],*(const u32x4*)(xp+32*t),c);
      u32x4 k0=pk8(c,0),k1=pk8(c,8);
      vmwait();
      const u32 o=(((s*H+h)*2+(m>>4))*14+7*j+w)*1024+((m&15)+16*hh)*16;
      ST128(k0,kq,o,16); ST128(k1,kq,o+512,16);
      char* q=kl+(s&1)*14336+w*2048+l*16; *(u32x4*)q=k0; *(u32x4*)(q+1024)=k1; } }
    else if(s>0){
      const char* xp=lds+b2+m*XROW+448*j+hh*16; const char* q=kl+((s-1)&1)*14336+l*16;
      #pragma unroll
      for(int t=0;t<14;t++) c=mf32(*(const u32x4*)(q+1024*t),*(const u32x4*)(xp+32*t),c);
      vmwait();
      const u32 o=(((s-1)*H+h)*2+j)*4096+hh*512+m*16;
      #pragma unroll
      for(int q4=0;q4<4;q4++) ST128(BC(u32x4,(f32x4{c[4*q4],c[4*q4+1],c[4*q4+2],c[4*q4+3]})),sxx,o+q4*1024,16);
    }
    if(s<3||s==NSEG) vmwait();
    __syncthreads();
    if(tid==0) ST32((u32)((s<3||s==NSEG)?s+1:s),flg,slot*128,16);
    int t=b2; b2=b0; b0=b1; b1=t;
  }
}

DEV void vo_worker(const ChainArgs& P,char* lds,int c,int w,int l,int tid){
  char* ws=P.ws; const int r=l&15,g=l>>4;
  auto xrs=mk(P.x,S*D*2); auto vor=mk(ws+O_VO,NSEG*NV*8*2048); auto flg=mk(ws+O_FLG,16384);
  XStage X; xs_init(X,tid); xs_load(X,xrs,0,tid);
  u32x4 W[2][14];
  #pragma unroll
  for(int hh=0;hh<2;hh++){ const char* p=ws+O_BT+wf(2*w+hh,16*c+r,g);
    #pragma unroll
    for(int t=0;t<14;t++) W[hh][t]=*(const u32x4*)(p+2048*t); }
  xs_put(X,lds,tid); xs_load(X,xrs,1,tid);
  __syncthreads();
  for(int s=0;s<NSEG;s++){
    xs_put(X,lds+((s+1)&1)*XBUF,tid); xs_load(X,xrs,min(s+2,NSEG-1),tid);
    f32x4 a[2][2]={{{0,0,0,0},{0,0,0,0}},{{0,0,0,0},{0,0,0,0}}};
    const char* xp=lds+(s&1)*XBUF+r*XROW+g*16;
    #pragma unroll
    for(int t=0;t<14;t++){
      #pragma unroll
      for(int hf=0;hf<2;hf++){ u32x4 xa=*(const u32x4*)(xp+hf*16*XROW+64*t); a[hf][0]=mf16(xa,W[0][t],a[hf][0]); a[hf][1]=mf16(xa,W[1][t],a[hf][1]); } }
    vmwait();
    #pragma unroll
    for(int hf=0;hf<2;hf++) ST128((u32x4{pk(a[hf][0][0],a[hf][0][1]),pk(a[hf][1][0],a[hf][1][1]),pk(a[hf][0][2],a[hf][0][3]),pk(a[hf][1][2],a[hf][1][3])}),vor,(((s*NV+c)*8+w)*2+hf)*1024+l*16,16);
    if(s<3||s==NSEG-1) vmwait();
    __syncthreads();
    if(tid==0) ST32((u32)((s<3||s==NSEG-1)?s+1:s),flg,(32+c)*128,16);
  }
}

DEV float sum16(float v){ v+=dpp<0xB1>(v); v+=dpp<0x4E>(v); v+=dpp<0x141>(v); v+=dpp<0x140>(v); return v; }
DEV float gelu(float a){ float u=a*(1.5957691216057308f+0.07135481282f*a*a); return a*__builtin_amdgcn_rcpf(1.f+__builtin_amdgcn_exp2f(-1.4426950408889634f*u)); }
DEV void mlp_worker(const ChainArgs& P,char* lds,int s,int w,int l,int tid){
  char* ws=P.ws; const int m=l&31,hh=l>>5;
  char* HL=lds+XBUF+256; float* GL=(float*)(lds+XBUF);
  { const int row=4*w+(l>>4), q=l&15; const u16* xr=P.x+(size_t)(s*32+row)*D;
    float v[4][8]; float sm=0,gd=0;
    #pragma unroll
    for(int i=0;i<4;i++){ const bool ok=(i<3||q<8); const int ch=ok?q+16*i:0; u32x4 a=*(const u32x4*)(xr+ch*8), b=*(const u32x4*)(P.gw+ch*8);
      #pragma unroll
      for(int k=0;k<4;k++){ u32 av=ok?a[k]:0u; u32 bv=b[k]; v[i][2*k]=lo16(av); v[i][2*k+1]=hi16(av); sm+=v[i][2*k]+v[i][2*k+1]; gd+=v[i][2*k]*lo16(bv)+v[i][2*k+1]*hi16(bv); } }
    sm=sum16(sm); gd=sum16(gd); const float mean=sm*(1.f/D); float vr=0;
    #pragma unroll
    for(int i=0;i<4;i++){ const bool ok=(i<3||q<8);
      #pragma unroll
      for(int k=0;k<8;k++){ v[i][k]-=mean; vr+=ok?v[i][k]*v[i][k]:0.f; } }
    vr=sum16(vr); const float rs=1.f/sqrtf(vr*(1.f/D)+1e-5f);
    #pragma unroll
    for(int i=0;i<4;i++) if(i<3||q<8) *(u32x4*)(lds+row*XROW+(q+16*i)*16)=u32x4{pk(v[i][0]*rs,v[i][1]*rs),pk(v[i][2]*rs,v[i][3]*rs),pk(v[i][4]*rs,v[i][5]*rs),pk(v[i][6]*rs,v[i][7]*rs)};
    const float gt=1.f/(1.f+__expf(-gd));
    if(q==0){ GL[row]=1.f-gt; ST32(BC(u32,gt),mk(ws+O_G,S*4),(s*32+row)*4,16); }
  }
  __syncthreads();
  { const char* yp=lds+m*XROW+hh*16;
    #pragma unroll 1
    for(int ft=w;ft<56;ft+=8){
      const char* p=ws+O_W1+(size_t)ft*28672+l*16; u32x4 Wt[28];
      #pragma unroll
      for(int t=0;t<28;t++) Wt[t]=*(const u32x4*)(p+1024*t);
      u32x4 ba=*(const u32x4*)(P.b1+32*ft+8*hh), bb=*(const u32x4*)(P.b1+32*ft+16+8*hh);
      f32x16 c;
      #pragma unroll
      for(int k=0;k<4;k++){ u32 x0=ba[k],x1=bb[k]; c[2*k]=lo16(x0); c[2*k+1]=hi16(x0); c[8+2*k]=lo16(x1); c[9+2*k]=hi16(x1); }
      #pragma unroll
      for(int t=0;t<28;t++) c=mf32(Wt[t],*(const u32x4*)(yp+32*t),c);
      #pragma unroll
      for(int k=0;k<16;k++) c[k]=gelu(c[k]);
      *(u32x4*)(HL+ft*2048+l*16)=pk8(c,0); *(u32x4*)(HL+ft*2048+1024+l*16)=pk8(c,8);
    } }
  __syncthreads();
  if(w<7){
    f32x16 c[2];
    #pragma unroll
    for(int i=0;i<2;i++){
      #pragma unroll
      for(int q=0;q<4;q++){ u32x2 b=*(const u32x2*)(P.b2+64*w+32*i+8*q+4*hh); u32 x0=b.x,x1=b.y; c[i][4*q]=lo16(x0); c[i][4*q+1]=hi16(x0); c[i][4*q+2]=lo16(x1); c[i][4*q+3]=hi16(x1); } }
    #pragma unroll 1
    for(int kb=0;kb<8;kb++){
      const char* p=ws+O_W2+(size_t)(2*w*112+kb*14)*1024+l*16; u32x4 Wt[2][14];
      #pragma unroll
      for(int t=0;t<14;t++){ Wt[0][t]=*(const u32x4*)(p+1024*t); Wt[1][t]=*(const u32x4*)(p+112*1024+1024*t); }
      #pragma unroll
      for(int t=0;t<14;t++){ u32x4 hb=*(const u32x4*)(HL+(kb*14+t)*1024+l*16); c[0]=mf32(Wt[0][t],hb,c[0]); c[1]=mf32(Wt[1][t],hb,c[1]); }
    }
    const float og=GL[m]; auto bbr=mk(ws+O_BB,S*D*4);
    #pragma unroll
    for(int i=0;i<2;i++){
      #pragma unroll
      for(int q=0;q<4;q++) ST128(BC(u32x4,(f32x4{c[i][4*q]*og,c[i][4*q+1]*og,c[i][4*q+2]*og,c[i][4*q+3]*og})),bbr,((s*112+16*w+8*i+2*q+hh)*32+m)*16,16); }
  }
  vmwait(); __syncthreads();
  if(tid==0) ST32(1u,mk(ws+O_FLG,16384),(64+s)*128,16);
}

DEV float max16x(float v){ auto r=__builtin_amdgcn_permlane16_swap(BC(u32,v),BC(u32,v),false,false); u32 a=r[0],b=r[1]; return vmax3(BC(float,a),BC(float,b),BC(float,b)); }
DEV float add16x(float v){ auto r=__builtin_amdgcn_permlane16_swap(BC(u32,v),BC(u32,v),false,false); u32 a=r[0],b=r[1]; return BC(float,a)+BC(float,b); }
DEV float max32x(float v){ auto r=__builtin_amdgcn_permlane32_swap(BC(u32,v),BC(u32,v),false,false); u32 a=r[0],b=r[1]; return vmax3(BC(float,a),BC(float,b),BC(float,b)); }
DEV void out_worker(const ChainArgs& P,char* lds,int sg,int cg,int w,int l,int tid){
  char* ws=P.ws; const int r=l&15,g=l>>4; int dead=SPIN_MAX;
  auto vor=mk(ws+O_VO,NSEG*NV*8*2048); auto flg=mk(ws+O_FLG,16384); auto sxx=mk(ws+O_SXX,NSEG*H*J*4096);
  auto sxm=mk(ws+O_SXM,NSEG*H*J*1024); auto upr=mk(ws+O_UP,NSEG*NV*8*512); auto bbr=mk(ws+O_BB,S*D*4); auto gr=mk(ws+O_G,S*4); auto outr=mk(P.out,S*D*2);
  #pragma unroll 1
  for(int s=sg;s<NSEG;s+=8){
    waitprog(flg,l<4?4*w+l:(l<8?32+4*cg+(l-4):64+s),l<4?s+2:(l<8?s+1:1),dead,16);
    f32x4 sv[2][2][2]; u32x4 vv[4][2];
    #pragma unroll
    for(int hh=0;hh<2;hh++){ const u32 o=((s*H+2*w+hh)*2)*4096+g*512+r*16;
      sv[hh][0][0]=BC(f32x4,LD128(sxx,o,16))+BC(f32x4,LD128(sxx,o+4096,16));
      sv[hh][1][0]=BC(f32x4,LD128(sxx,o+256,16))+BC(f32x4,LD128(sxx,o+4096+256,16));
      sv[hh][1][1]=BC(f32x4,LD128(sxx,o+2048+256,16))+BC(f32x4,LD128(sxx,o+4096+2048+256,16)); }
    #pragma unroll
    for(int ct=0;ct<4;ct++){ const u32 o=((s*NV+4*cg+ct)*8+w)*2048+l*16; vv[ct][0]=LD128(vor,o,16); vv[ct][1]=LD128(vor,o+1024,16); }
    const int ort=w>>2, oct=w&3;
    const f32x4 bb=BC(f32x4,LD128(bbr,((s*112+16*cg+4*oct+g)*32+16*ort+r)*16,16));
    const float gt=BC(float,__builtin_amdgcn_raw_buffer_load_b32(gr,(s*32+16*ort+r)*4,0,16));
    f32x4 tmp[2][2][4]; float mx[2],lx[2];
    #pragma unroll
    for(int hh=0;hh<2;hh++){
      #pragma unroll
      for(int rt=0;rt<2;rt++){
        f32x4 a=sv[hh][rt][0], b=sv[hh][rt][rt];
        #pragma unroll
        for(int i=0;i<4;i++) b[i]=(4*g+i<=r)?b[i]:-3.0e38f;
        float mm=vmax3(vmax3(b[0],b[1],b[2]),b[3],b[3]); if(rt) mm=vmax3(vmax3(a[0],a[1],mm),a[2],a[3]);
        mm=max32x(max16x(mm)); const float mc=mm*CSC; float sum=0;
        #pragma unroll
        for(int i=0;i<4;i++){ b[i]=(4*g+i<=r)?__builtin_amdgcn_exp2f(b[i]*CSC-mc):0.f; sum+=b[i]; if(rt){ a[i]=__builtin_amdgcn_exp2f(a[i]*CSC-mc); sum+=a[i]; } }
        sum=add16x(sum); sum=swadd(sum,sum);
        if(hh==0||g>=2){ mx[rt]=mc; lx[rt]=sum; }
        const u32x4 e= rt? u32x4{pk(a[0],a[1]),pk(a[2],a[3]),pk(b[0],b[1]),pk(b[2],b[3])} : u32x4{pk(b[0],b[1]),pk(b[2],b[3]),0u,0u};
        #pragma unroll
        for(int ct=0;ct<4;ct++) tmp[hh][rt][ct]=mf16(u32x4{vv[ct][0][hh],vv[ct][0][2+hh],vv[ct][1][hh],vv[ct][1][2+hh]},e,f32x4{0,0,0,0});
      } }
    const u32 om=((s*H+2*w+(g>>1))*2)*1024+(g&1)*256+r*16, ou=((s*NV+4*cg)*8+w)*512+l*8;
    { u32 v;
      #pragma unroll 1
      while(true){ asm volatile("":::"memory"); v=__builtin_amdgcn_raw_buffer_load_b32(sxm,((s*H+2*w)*2)*1024,0,16); if(__builtin_amdgcn_readfirstlane(v)!=SENT || --dead<=0) break; nap(4); } }
    u32x4 q[2][2]; u32x2 uu[4];
    #pragma unroll 1
    while(true){ asm volatile("":::"memory"); u32 mxs=0;
      #pragma unroll
      for(int rt=0;rt<2;rt++){ q[rt][0]=LD128(sxm,om+rt*512,16); q[rt][1]=LD128(sxm,om+1024+rt*512,16); }
      #pragma unroll
      for(int ct=0;ct<4;ct++) uu[ct]=LD64(upr,ou+ct*4096,16);
      #pragma unroll
      for(int rt=0;rt<2;rt++){ mxs=umax3(umax3(mxs,q[rt][0].x,q[rt][0].y),q[rt][0].z,q[rt][0].w); mxs=umax3(umax3(mxs,q[rt][1].x,q[rt][1].y),q[rt][1].z,q[rt][1].w); }
      #pragma unroll
      for(int ct=0;ct<4;ct++) mxs=umax3(mxs,uu[ct].x,uu[ct].y);
      if(!anysent(mxs) || --dead<=0) break; nap(2); }
    #pragma unroll
    for(int rt=0;rt<2;rt++){
      f32x4 t=(BC(f32x4,q[rt][0])+BC(f32x4,q[rt][1]))*CSC;
      float mm=max16x(vmax3(vmax3(t[0],t[1],t[2]),t[3],mx[rt]));
      float a0=__builtin_amdgcn_exp2f(t[0]-mm),a1=__builtin_amdgcn_exp2f(t[1]-mm),a2=__builtin_amdgcn_exp2f(t[2]-mm),a3=__builtin_amdgcn_exp2f(t[3]-mm),f=__builtin_amdgcn_exp2f(mx[rt]-mm);
      const float rl=__builtin_amdgcn_rcpf(add16x((a0+a1)+(a2+a3))+f*lx[rt]), sc=f*rl;
      auto sp=__builtin_amdgcn_permlane32_swap(BC(u32,sc),BC(u32,sc),false,false); u32 s0=sp[0],s1=sp[1]; const float sA=BC(float,s0),sB=BC(float,s1);
      const u32x2 pm={pk(a0*rl,a1*rl),pk(a2*rl,a3*rl)};
      #pragma unroll
      for(int ct=0;ct<4;ct++) *(f32x4*)(lds+((w*8+rt*4+ct)*64+l)*16)=mf16k(uu[ct],pm,tmp[0][rt][ct]*sA+tmp[1][rt][ct]*sB);
    }
    __syncthreads();
    f32x4 o=*(const f32x4*)(lds+(w*64+l)*16);
    #pragma unroll
    for(int k=1;k<8;k++) o+=*(const f32x4*)(lds+((k*8+w)*64+l)*16);
    o=o*gt+bb;
    ST64((u32x2{pk(o[0],o[1]),pk(o[2],o[3])}),outr,((s*32+16*ort+r)*D+64*cg+16*oct+4*g)*2,0);
    __syncthreads();
  }
}

constexpr int LDS2=XBUF+256+114688;
__global__ __launch_bounds__(512,2) void k_main(ChainArgs P){
  extern __shared__ __attribute__((aligned(16))) char lds[];
  const int b=blockIdx.x, xcd=b&7, slot=b>>3;
  const int tid=threadIdx.x, l=tid&63, w=__builtin_amdgcn_readfirstlane(tid>>6);
  if(xcd==0){
    if(w==0) score_loop<0>(P,lds,slot,w,l);
    else if(w<3) score_loop<1>(P,lds,slot,w,l);
    else if(w<7) score_loop<2>(P,lds,slot,w,l);
    else if((slot&1)==0) score_loop<3>(P,lds,slot,w,l);
    else score_loop<4>(P,lds,slot,w,l);
  } else if(xcd==2){ if(slot<NV){ if(w<4) value_loop<1>(P,lds,slot,w,l); else value_loop<0>(P,lds,slot,w,l); } }
  else if(xcd==1){ if(w<7) kq_worker<0>(P,lds,slot,w,l,tid); else kq_worker<1>(P,lds,slot,w,l,tid); }
  else if(xcd==3){ if(slot<NV) vo_worker(P,lds,slot,w,l,tid); }
  else if(xcd<6) mlp_worker(P,lds,slot*2+(xcd-4),w,l,tid);
  else if(slot<28) out_worker(P,lds,(slot/7)*2+(xcd-6),slot%7,w,l,tid);
}

struct PreArgs{ const u16* mem0; const u16* Wq; const u16* Wk; const u16* Wv; const u16* Wo; const u16* w1; const u16* w2; char* ws; };
template<int WH>
DEV void ab_gemm(const PreArgs& P,char* lds,int h,int mb,int w,int l,int tid){
  const int m=l&31,hh=l>>5; u32x4 st[4][8];
  const u16* Np=(WH?P.Wv:P.Wk)+(size_t)(tid>>3)*4096+h*256+(tid&7)*8;
  const u16* Mp= WH? P.Wo+(size_t)(h*256+(tid>>3))*D+64*mb+(tid&7)*8 : P.Wq+(size_t)(64*mb+(tid>>3))*4096+h*256+(tid&7)*8;
  #pragma unroll
  for(int q=0;q<4;q++){
    #pragma unroll
    for(int i=0;i<7;i++) st[q][i]=*(const u32x4*)(Np+(size_t)i*64*4096+64*q);
    st[q][7]=*(const u32x4*)(Mp+(WH?64*D:64)*q); }
  const int la=(tid>>3)*128+(((tid&7)^((tid>>4)&7))*16), lam=WH?57344+tid*16:57344+la;
  f32x16 c[2][2];
  #pragma unroll
  for(int i=0;i<16;i++){ c[0][0][i]=0;c[0][1][i]=0;c[1][0][i]=0;c[1][1][i]=0; }
  const int ra=sw23(m);
  #pragma unroll
  for(int hk=0;hk<2;hk++){
    if(hk) __syncthreads();
    #pragma unroll
    for(int q=0;q<2;q++){
      #pragma unroll
      for(int i=0;i<7;i++) *(u32x4*)(lds+q*65536+i*8192+la)=st[2*hk+q][i];
      *(u32x4*)(lds+q*65536+lam)=st[2*hk+q][7]; }
    __syncthreads();
    if(w<7){
      #pragma unroll
      for(int q=0;q<2;q++){
        #pragma unroll
        for(int t=0;t<4;t++){ u32x4 a[2],b[2];
          #pragma unroll
          for(int i=0;i<2;i++){
            a[i]=*(const u32x4*)(lds+q*65536+(64*w+32*i+ra)*128+(((2*t+hh)^((ra>>1)&7))*16));
            if(WH){ const char* p=lds+q*65536+57344+(16*t+8*hh+((l&15)>>2))*128+(32*i+16*((l>>4)&1)+4*(l&3))*2; u32x2 x0=trread(p),x1=trread(p+512); b[i]=u32x4{x0.x,x0.y,x1.x,x1.y}; }
            else b[i]=*(const u32x4*)(lds+q*65536+57344+(32*i+m)*128+(((2*t+hh)^((m>>1)&7))*16)); }
          c[0][0]=mf32(a[0],b[0],c[0][0]); c[0][1]=mf32(a[0],b[1],c[0][1]); c[1][0]=mf32(a[1],b[0],c[1][0]); c[1][1]=mf32(a[1],b[1],c[1][1]);
        } } } }
  if(w<7){ char* o=P.ws+(WH?O_BT:O_A);
    #pragma unroll
    for(int ti=0;ti<2;ti++){
      #pragma unroll
      for(int tj=0;tj<2;tj++){ char* p=o+wf(h,64*mb+32*tj+m,8*w+4*ti+hh); *(u32x4*)p=pk8(c[ti][tj],0); *(u32x4*)(p+1024)=pk8(c[ti][tj],8); } } }
}
__global__ __launch_bounds__(512,2) void k_pre(PreArgs P){
  extern __shared__ __attribute__((aligned(16))) char lds[];
  const int b=blockIdx.x, xcd=b&7, slot=b>>3;
  const int tid=threadIdx.x, l=tid&63, w=__builtin_amdgcn_readfirstlane(tid>>6);
  char* ws=P.ws; const u32x4 sent={SENT,SENT,SENT,SENT}; auto hot=mk(ws+O_HOT,66*65536);
  if(slot<28){ const int gm=xcd*4+slot/7; if(gm&1) ab_gemm<1>(P,lds,gm>>1,slot%7,w,l,tid); else ab_gemm<0>(P,lds,gm>>1,slot%7,w,l,tid); }
  else {
    const int mi=(slot-28)*8+xcd;
    if(xcd==0 && (slot-28)*8+w<9){
      const int i=(slot-28)*8+w; u32 ta=~0u,tb=~0u;
      for(int rep=0;rep<3;rep++){ u32* p=(u32*)(ws+O_HOT+(size_t)i*524288+49152+rep*256); u32* p2=p+2048; u32 v=0;
        asm volatile("global_store_dword %0, %2, off sc1\n global_store_dword %1, %2, off sc1\n s_waitcnt vmcnt(0)"::"v"(p),"v"(p2),"v"(v):"memory");
        u32 t0=now(); asm volatile("global_load_dword %0, %1, off sc1\n s_waitcnt vmcnt(0)":"=v"(v):"v"(p):"memory");
        u32 t1=now(); asm volatile("global_load_dword %0, %1, off sc1\n s_waitcnt vmcnt(0)":"=v"(v):"v"(p2):"memory");
        u32 t2=now(); ta=min(ta,t1-t0); tb=min(tb,t2-t1); }
      if(l==0) ((u32*)(ws+O_XOR))[i]=tb<ta?8192u:0u;
    }
    if(mi==1){ for(int k=tid;k<1792;k+=512){ int c=k>>6, hi=(k>>5)&1, i=(k>>2)&7, q=k&3; u32 v=canon(*(const u32*)(P.mem0+i*D+16*c+4*q+2*hi)); ST32(v,hot,k*4,16); ST32(v,hot,8192+k*4,16); }
      if(tid<128) ST32(0u,mk(ws+O_FLG,16384),tid*128,16); }
    for(int id=mi*512+tid;id<2*100352;id+=32*512){ const bool two=id>=100352; const int k=two?id-100352:id, ln=k&63, mm=ln&31, h2=ln>>5, fr=k>>6;
      const u16* p; int str;
      if(!two){ p=P.w1+(size_t)(16*(fr%28)+8*h2)*F+32*(fr/28)+sw23(mm); str=F; } else { p=P.w2+(size_t)(16*(fr%112)+8*h2)*D+32*(fr/112)+mm; str=D; }
      u32 e[8];
      #pragma unroll
      for(int j=0;j<8;j++) e[j]=p[j*str];
      *(u32x4*)(ws+O_W1+(size_t)id*16)=u32x4{e[0]|(e[1]<<16),e[2]|(e[3]<<16),e[4]|(e[5]<<16),e[6]|(e[7]<<16)}; }
  }
  for(u32 i=b*512+tid;i<65*3072;i+=256*512){ u32 blk=i/3072, k=i%3072; if(i>=1024) ST128(sent,hot,blk*65536+k*16,16); }
  for(u32 i=b*512+tid;i<(O_SXX-O_UP)/16;i+=256*512) ST128(sent,mk(ws+O_UP,(u32)(O_SXX-O_UP)),i*16,16);
}
extern "C" void kernel_launch(void* const* d_in,const int* in_sizes,int n_in,void* d_out,int out_size,void* d_ws,size_t ws_size,hipStream_t stream){
  char* ws=(char*)d_ws;
  (void)hipFuncSetAttribute((const void*)k_pre,hipFuncAttributeMaxDynamicSharedMemorySize,131072);
  (void)hipFuncSetAttribute((const void*)k_main,hipFuncAttributeMaxDynamicSharedMemorySize,LDS2);
  PreArgs Q{(const u16*)d_in[1],(const u16*)d_in[2],(const u16*)d_in[3],(const u16*)d_in[4],(const u16*)d_in[5],(const u16*)d_in[8],(const u16*)d_in[10],ws};
  k_pre<<<256,512,131072,stream>>>(Q);
  ChainArgs P{(const u16*)d_in[0],ws,(const u16*)d_in[9],(const u16*)d_in[11],(const u16*)d_in[12],(u16*)d_out,8,14,8,8,0,0};
  k_main<<<256,512,LDS2,stream>>>(P);
}
```

```cpp
#include <hip/hip_runtime.h>
#include <hip/hip_bf16.h>
typedef unsigned u32; typedef unsigned long long u64; typedef unsigned short u16;
typedef u32 u32x2 __attribute__((ext_vector_type(2)));
typedef u32 u32x4 __attribute__((ext_vector_type(4)));
typedef u32 u32x8 __attribute__((ext_vector_type(8)));
typedef float f32x2 __attribute__((ext_vector_type(2)));
typedef float f32x4 __attribute__((ext_vector_type(4)));
typedef __bf16 bf16x2 __attribute__((ext_vector_type(2)));
typedef __bf16 bf16x8 __attribute__((ext_vector_type(8)));
typedef __bf16 bf16x16 __attribute__((ext_vector_type(16)));
typedef float f32x16 __attribute__((ext_vector_type(16)));
typedef short s16x4 __attribute__((ext_vector_type(4)));
typedef short s16x8 __attribute__((ext_vector_type(8)));
#define DEV __device__ __forceinline__
template<class T,class U> __device__ __forceinline__ T bcf(U x){ return __builtin_bit_cast(T,x); }
#define BC(T,x) bcf<T>(x)

constexpr int D=448, H=16, DH=256, S=2048, NSEG=64, F=1792, J=2, NV=28;
constexpr float CSC = 1.4426950408889634f/16.0f;
constexpr size_t O_HOT=0;
constexpr size_t O_XOR=O_HOT+(size_t)66*65536;
constexpr size_t O_FLG=O_XOR+32768;
constexpr size_t O_A  =O_XOR+65536;
constexpr size_t O_BT =O_A+(size_t)H*D*D*2;
constexpr size_t O_KQ =O_BT+(size_t)H*D*D*2;
constexpr size_t O_VO =O_KQ+(size_t)NSEG*H*14*2048;
constexpr size_t O_UP =O_VO+(size_t)NSEG*NV*8*2048;
constexpr size_t O_SXM=O_UP+(size_t)NSEG*NV*8*512;
constexpr size_t O_SXX=O_SXM+(size_t)NSEG*H*J*1024;
constexpr size_t O_BB =O_SXX+(size_t)NSEG*H*J*4096;
constexpr size_t O_G  =O_BB+(size_t)S*D*4;
constexpr size_t O_W1 =O_G+(size_t)S*4;
constexpr size_t O_W2 =O_W1+(size_t)D*F*2;
constexpr size_t O_END=O_W2+(size_t)D*F*2;

DEV float bf2f(u16 v){ return BC(float,(u32)v<<16); }
DEV u16 f2bf(float f){ __bf16 b=(__bf16)f; return BC(u16,b); }
DEV u32 pk(float a,float b){ bf16x2 v={(__bf16)a,(__bf16)b}; return BC(u32,v); }
DEV __amdgpu_buffer_rsrc_t mk(const void* p, u32 bytes){ return __builtin_amdgcn_make_buffer_rsrc((void*)p,0,bytes,0x00020000); }
DEV u32 now(){ u64 t; asm volatile("s_memtime %0\n s_waitcnt lgkmcnt(0)":"=s"(t)::"memory"); return (u32)t; }
DEV f32x4 smf64(u32x4 a,u32x8 b,f32x4 c,int idx){ return __builtin_amdgcn_smfmac_f32_16x16x64_bf16(BC(bf16x8,a),BC(bf16x16,b),c,idx,0,0); }
DEV f32x4 smf32(u32x2 a,u32x4 b,f32x4 c,int idx){ return __builtin_amdgcn_smfmac_f32_16x16x32_bf16(BC(s16x4,a),BC(s16x8,b),c,idx,0,0); }
DEV float swadd(float a,float b){ auto r=__builtin_amdgcn_permlane32_swap(BC(u32,a),BC(u32,b),false,false); return BC(float,r[0])+BC(float,r[1]); }
template<int C> DEV float dpp(float x){ return BC(float,__builtin_amdgcn_update_dpp(0,BC(int,x),C,0xF,0xF,true)); }
DEV float vmax3(float a,float b,float c){ float r; asm("v_max3_f32 %0,%1,%2,%3":"=v"(r):"v"(a),"v"(b),"v"(c)); return r; }
template<int C> DEV float maxdpp(float x){ float y=dpp<C>(x); return vmax3(x,y,y); }
DEV u32x8 cat(u32x4 a,u32x4 b){ return __builtin_shufflevector(a,b,0,1,2,3,4,5,6,7); }
DEV u32 canon(u32 x){ return x>0xFFFFFFFEu?0xFFFFFFFEu:x; }
DEV u32x2 trread(const char* p){ return BC(u32x2,__builtin_amdgcn_ds_read_tr16_b64_v4i16((__attribute__((address_space(3))) s16x4*)p)); }
constexpr int SPIN_MAX=400000;
#define SENT 0xFFFFFFFFu
DEV u32 umax3(u32 a,u32 b,u32 c){ return max(max(a,b),c); }
DEV bool anysent(u32 m){ return __builtin_amdgcn_ballot_w64(m==SENT)!=0; }
DEV void nap(int n){ for(int i=0;i<n;i++) __builtin_amdgcn_s_sleep(1); }
DEV void until(u32 t){ int d; while((d=(int)(t-now()))>0){ if(d>160) __builtin_amdgcn_s_sleep(1); } }
DEV u32 max8(u32x4 a,u32x4 b){ return max(umax3(umax3(a.x,a.y,a.z),a.w,b.x),umax3(b.y,b.z,b.w)); }
DEV u32x4 poll16(__amdgpu_buffer_rsrc_t r,u32 off,int& budget){
  u32x4 v;
  #pragma unroll 1
  do { asm volatile("":::"memory"); v=__builtin_amdgcn_raw_buffer_load_b128(r,off,0,16);
  } while(anysent(max(umax3(v.x,v.y,v.z),v.w)) && --budget>0);
  return v;
}
DEV u32 loadX(const char* ws,int cls){ const u32* xr=(const u32*)(ws+O_XOR)+16+cls*8; u32 X=0; for(int i=0;i<8;i++) X|=(xr[i]&3u)<<(2*i); return __builtin_amdgcn_readfirstlane(X); }
#define XM(s) (((XX>>(((s)>>3)*2))&3u)<<13)

DEV f32x16 mf32(u32x4 a,u32x4 b,f32x16 c){ return __builtin_amdgcn_mfma_f32_32x32x16_bf16(BC(bf16x8,a),BC(bf16x8,b),c,0,0,0); }
DEV f32x4 mf16(u32x4 a,u32x4 b,f32x4 c){ return __builtin_amdgcn_mfma_f32_16x16x32_bf16(BC(bf16x8,a),BC(bf16x8,b),c,0,0,0); }
DEV f32x4 mf16k(u32x2 a,u32x2 b,f32x4 c){ return __builtin_amdgcn_mfma_f32_16x16x16bf16_1k(BC(s16x4,a),BC(s16x4,b),c,0,0,0); }
DEV int sw23(int m){ return (m&19)|((m&4)<<1)|((m&8)>>1); }
DEV u32 wf(int h,int row,int kc){ return (u32)(((h*14+(row>>5))*56+kc)*512+(row&31)*16); }
DEV void vmwait(){ asm volatile("s_waitcnt vmcnt(0)":::"memory"); }
DEV float lo16(u32 v){ return BC(float,v<<16); }
DEV float hi16(u32 v){ return BC(float,v&0xFFFF0000u); }
DEV int waitprog(__amdgpu_buffer_rsrc_t fl,int idx,int need,int& budget,int slp){
  int v;
  #pragma unroll 1
  while(true){ asm volatile("":::"memory"); v=(int)__builtin_amdgcn_raw_buffer_load_b32(fl,idx*128,0,16);
    if(__builtin_amdgcn_ballot_w64(v<need)==0 || --budget<=0) break; nap(slp); }
  return v;
}

struct ChainArgs{ const u16* x; char* ws; const u16* b1; const u16* b2; const u16* gw; u16* out; int dA; int dB; int dC; int dD; int dE; int dX; int dG; int ts; };
constexpr int L_MEM=0, L_TT=7168, L_SX=L_TT+2*2112+64, L_L=L_SX+1024, L_RED=7168, L_U=L_RED+4096;

#define LD128(r,o,aux) __builtin_amdgcn_raw_buffer_load_b128(r,o,0,aux)
#define LD32(r,o,aux) __builtin_amdgcn_raw_buffer_load_b32(r,o,0,aux)
#define LD64(r,o,aux) __builtin_amdgcn_raw_buffer_load_b64(r,o,0,aux)
#define ST64(v,r,o,aux) __builtin_amdgcn_raw_buffer_store_b64(v,r,o,0,aux)
#define ST32(v,r,o,aux) __builtin_amdgcn_raw_buffer_store_b32(v,r,o,0,aux)

template<int ROLE>
DEV void score_loop(const ChainArgs& P,char* lds,int slot,int w,int l,int cls){
  const int r=l&15,g=l>>4; char* ws=P.ws;
  const int h=slot>>1, j=slot&1, n0=224*j;
  auto hot=mk(ws+O_HOT,66*65536);
  const u32 XX=loadX(ws,cls);
  const int idx=(r<8)?0x4444:0xEEEE;
  int dead=SPIN_MAX;
  u32x8 W[2][7];
  auto kq=mk(ws+O_KQ,NSEG*H*14*2048); auto flg=mk(ws+O_FLG,16384); int have=0;
  if(ROLE<3){
    #pragma unroll
    for(int tt=0;tt<2;tt++){ const char* p=ws+O_A+wf(h,n0+32*w+16*tt+r,g);
      #pragma unroll
      for(int t=0;t<7;t++) W[tt][t]=cat(*(const u32x4*)(p+4096*t),*(const u32x4*)(p+4096*t+2048)); }
  } else if(ROLE==3){
    { int v=waitprog(flg,2*h+(l&1),1,dead,1); have=min(__builtin_amdgcn_readlane(v,0),__builtin_amdgcn_readlane(v,1)); }
    #pragma unroll
    for(int k=0;k<14;k++){ u32 o=(u32)h*14*2048+k*2048+l*16; W[k/7][k%7]=cat(LD128(kq,o,16),LD128(kq,o+1024,16)); }
    __builtin_amdgcn_s_setprio(3);
  }
  if(ROLE==0){ *(u32x2*)(lds+L_TT+(l>>5)*2112+(224+(l&31))*8)=u32x2{0,0}; }
  if(ROLE==0) __builtin_amdgcn_s_setprio(2);
  u32x4 xb[7];
  auto xrs=mk(P.x,S*D*2);
  const u32 xo=(u32)((16*(w-1)+r)*D+n0+8*g)*2;
  if(ROLE==1){
    #pragma unroll
    for(int k=0;k<7;k++) xb[k]=LD128(xrs,xo+64*k,0);
  }
  const int rr=r>>2, hp=(l>>1)&1, q1=l&1;
  const char* trp=lds+L_TT+q1*2112+(32*(g>>1)+16*(g&1)+(ROLE==0?4*hp+rr:2*hp+(rr&1)+4*(rr>>1)))*8;
  auto sxm=mk(ws+O_SXM,NSEG*H*J*1024);
  __builtin_amdgcn_s_waitcnt(0x0F70);

  u32 dl=now();
  for(int s=0;s<NSEG;s++){
    const u32 X=XM(s); const u32 base=s*65536u;
    if(ROLE<3){ until(dl); u32x4 v=poll16(hot,(s?base:64*65536u)+((w*1024+l*16)^(s?X:0u)),dead); if(s) {  }  *(u32x4*)(lds+L_MEM+w*1024+l*16)=v; }
    __syncthreads();
    if(ROLE==4){  } else {  }
    float v0,v1,v2,v3,mc; u32x2 mb[14]; u32x4 am[7];
    if(ROLE<4){
      f32x4 c0={0,0,0,0},c1={0,0,0,0};
      #pragma unroll
      for(int t=0;t<7;t++) am[t]=*(const u32x4*)(lds+L_MEM+t*1024+l*16);
      __builtin_amdgcn_sched_barrier(0);
      #pragma unroll
      for(int t=0;t<7;t++){ c0=smf64(am[t],W[0][t],c0,idx); c1=smf64(am[t],W[1][t],c1,idx); }
      if(ROLE==0){
        #pragma unroll
        for(int k=0;k<7;k++){ const char* p=lds+L_MEM+(14*j+2*k+(g>>1))*256+(l&7)*16+(g&1)*8; mb[2*k]=*(const u32x2*)p; mb[2*k+1]=*(const u32x2*)(p+128); }
      }
      v0=swadd(c0[0],c1[0]);v1=swadd(c0[1],c1[1]);v2=swadd(c0[2],c1[2]);v3=swadd(c0[3],c1[3]);
      asm volatile("":"+v"(v0),"+v"(v3));
      if(ROLE<3) *(u32x2*)(lds+L_TT+(g&1)*2112+(32*w+16*(l>>5)+r)*8)=u32x2{pk(v0,v1),pk(v2,v3)};
      if(ROLE==3){
        float* sx=(float*)(lds+L_SX)+(4*(g&1))*32+16*(l>>5)+r;
        sx[0]=v0; sx[32]=v1; sx[64]=v2; sx[96]=v3;
        __builtin_amdgcn_wave_barrier();
        f32x4 z=*(const f32x4*)(lds+L_SX+l*16);
        float m=vmax3(vmax3(z[0],z[1],z[2]),z[3],z[3]);
        m=maxdpp<0xB1>(m); m=maxdpp<0x4E>(m); m=maxdpp<0x141>(m);
        mc=m*CSC;
        v0=__builtin_amdgcn_exp2f(z[0]*CSC-mc);v1=__builtin_amdgcn_exp2f(z[1]*CSC-mc);v2=__builtin_amdgcn_exp2f(z[2]*CSC-mc);v3=__builtin_amdgcn_exp2f(z[3]*CSC-mc);
        asm volatile("":"+v"(v0),"+v"(v3));
      }
    }
    __syncthreads();
    if(ROLE<4){  }
    if(ROLE==0){
      if(s<NSEG-1){
        f32x4 c={0,0,0,0},c2={0,0,0,0}; u32x2 ta[8];
        #pragma unroll
        for(int u=0;u<8;u++) ta[u]=trread(trp+(u>>1)*512+(u&1)*64);
        __builtin_amdgcn_sched_barrier(0);
        #pragma unroll
        for(int u=0;u<4;u++){
          u32x2 a0=ta[2*u], a1=ta[2*u+1];
          u32x4 f0={mb[4*u].x,mb[4*u].y,mb[4*u+1].x,mb[4*u+1].y};
          u32x4 f1= u<3? u32x4{mb[(4*u+2)%14].x,mb[(4*u+2)%14].y,mb[(4*u+3)%14].x,mb[(4*u+3)%14].y} : u32x4{0,0,0,0};
          if(u&1) c2=smf64(u32x4{a0.x,a0.y,a1.x,a1.y},cat(f0,f1),c2,idx); else c=smf64(u32x4{a0.x,a0.y,a1.x,a1.y},cat(f0,f1),c,idx);
        }
        __builtin_amdgcn_sched_barrier(0);
        c+=c2; float sa=swadd(c[0],c[2])*CSC, sb=swadd(c[1],c[3])*CSC;
        if(r<8) ST64((u32x2{canon(BC(u32,sa)),canon(BC(u32,sb))}),hot,base+32768+(((h*J+j)*256+(g>>1)*128+r*16+(g&1)*8)^X),16);
      }

      dl=now()+P.dA;
    } else if(ROLE==2){ dl=now()+P.dD;
    } else if(ROLE==1){
      dl=now(); until(dl+P.dE); dl+=P.dD;
      f32x4 c={0,0,0,0};
      #pragma unroll
      for(int u=0;u<4;u++){
        u32x2 a0=trread(trp+u*512), a1=trread(trp+u*512+64);
        c=smf64(u32x4{a0.x,a0.y,a1.x,a1.y},cat(xb[2*u],u<3?xb[(2*u+1)%7]:u32x4{0,0,0,0}),c,idx);
      }
      float sa=swadd(c[0],c[2]), sb=swadd(c[1],c[3]);
      ST64((u32x2{canon(BC(u32,sa)),canon(BC(u32,sb))}),sxm,((s*H+h)*J+j)*1024+(w-1)*512+(g&1)*256+r*16+(g>>1)*8,16);
      const u32 xn=xo+min(s+1,NSEG-1)*32*D*2;
      #pragma unroll
      for(int k=0;k<7;k++) xb[k]=LD128(xrs,xn+64*k,0);
    } else if(ROLE==3 && s<NSEG-1){
      float sm=(v0+v1)+(v2+v3);
      sm+=dpp<0xB1>(sm); sm+=dpp<0x4E>(sm); sm+=dpp<0x141>(sm);
      float rl=__builtin_amdgcn_rcpf(sm);
      ST64((u32x2{canon(pk(v0*rl,v1*rl)),canon(pk(v2*rl,v3*rl))}),hot,base+((16384+h*512+l*8)^X),16);
      if((l&7)==0) ST32(canon(BC(u32,mc+__builtin_amdgcn_logf(sm))),hot,base+((7168+h*32+(l>>3)*4)^X),16);

      until(now()+P.dE);
      if(have<s+2){ int v=waitprog(flg,2*h+(l&1),s+2,dead,1); have=min(__builtin_amdgcn_readlane(v,0),__builtin_amdgcn_readlane(v,1)); }
      const u32 o=(u32)((s+1)*H+h)*14*2048+l*16;
      #pragma unroll
      for(int k=0;k<14;k++) W[k/7][k%7]=cat(LD128(kq,o+k*2048,16),LD128(kq,o+k*2048+1024,16));
    }
  }

}

template<int FIN>
DEV void value_loop(const ChainArgs& P,char* lds,int c,int w,int l,int cls){
  const int r=l&15,g=l>>4; char* ws=P.ws;
  auto hot=mk(ws+O_HOT,66*65536);
  const u32 XX=loadX(ws,cls);
  const int idx=(r<8)?0x4444:0xEEEE;
  int dead=SPIN_MAX;
  const int pr=FIN?2*w:2*(w-4)+1;
  u32x8 W[2][7];
  #pragma unroll
  for(int hh=0;hh<2;hh++){ const char* p=ws+O_BT+wf(2*pr+hh,16*c+r,g);
    #pragma unroll
    for(int t=0;t<7;t++) W[hh][t]=cat(*(const u32x4*)(p+4096*t),*(const u32x4*)(p+4096*t+2048)); }
  auto flg=mk(ws+O_FLG,16384); int have=0;
  auto vor=mk(ws+O_VO,NSEG*NV*8*2048);
  auto upr=mk(ws+O_UP,NSEG*NV*8*512);
  u32 voo=(c*8+pr)*2048+l*16;
  u32x8 vo0,vo1;
  if(FIN) __builtin_amdgcn_s_setprio(2);
  const int i=l&7, hi=(l>>3)&1;
  const u32 dm0=(i==2*hi?0xFFFFu:0u)|(i==2*hi+1?0xFFFF0000u:0u), dm1=(i==4+2*hi?0xFFFFu:0u)|(i==5+2*hi?0xFFFF0000u:0u);
  const u32 oEX=16384+(4*w+hi)*512+i*64+g*16, oSM=(4*w+g)*J*256+hi*128+i*16, oL=7168+(4*w+g)*32+i*4;
  float* redw=(float*)(lds+L_RED)+w*144+((r>>1)&1)*72+(4*(g&1)+2*(l>>5))*8+(r>>2)*2+(r&1);
  const char* redr=lds+L_RED+((l>>5)*72+(l&31)*2)*4;
  char* ux=lds+L_U+(w&3)*512+l*8;
  __builtin_amdgcn_s_waitcnt(0x0F70);

  u32 dl=now();
  for(int s=0;s<NSEG;s++){
    const u32 X=XM(s), X1=XM(s+1); const u32 base=s*65536u;
    if(!FIN){ until(dl); const u32 mb=s?base:64*65536u, mx=s?X:0u; const u32 o0=mb+(((w-4)*2048+l*16)^mx), o1=mb+((min((w-4)*2048+1024,6144)+l*16)^mx); u32x4 v0,v1;
      #pragma unroll 1
      do{ asm volatile("":::"memory"); v0=LD128(hot,o0,16); v1=LD128(hot,o1,16);
      } while(anysent(max(umax3(umax3(v0.x,v0.y,v0.z),v0.w,v1.x),umax3(v1.y,v1.z,v1.w))) && --dead>0);

      *(u32x4*)(lds+L_MEM+(w-4)*2048+l*16)=v0; if(w<7) *(u32x4*)(lds+L_MEM+(w-4)*2048+1024+l*16)=v1; }
    __syncthreads();

    if(FIN){ dl=now()+P.dB;
      if(s<NSEG-1){ if(have<s+1) have=__builtin_amdgcn_readfirstlane(waitprog(flg,32+c,s+1,dead,1));
        vo0=cat(LD128(vor,voo,16),LD128(vor,voo+1024,16)); vo1=cat(LD128(vor,voo+2048,16),LD128(vor,voo+3072,16)); voo+=NV*8*2048; } }
    f32x4 c0={0,0,0,0},c1={0,0,0,0}; u32x4 am[7];
    #pragma unroll
    for(int t=0;t<7;t++) am[t]=*(const u32x4*)(lds+L_MEM+t*1024+l*16);
    __builtin_amdgcn_sched_barrier(0);
    #pragma unroll
    for(int t=0;t<7;t++){ c0=smf64(am[t],W[0][t],c0,idx); c1=smf64(am[t],W[1][t],c1,idx); }
    u32x2 u={canon(pk(swadd(c0[0],c1[0]),swadd(c0[1],c1[1]))), canon(pk(swadd(c0[2],c1[2]),swadd(c0[3],c1[3])))};
    ST64(u,upr,((s*NV+c)*8+pr)*512+l*8,16);
    if(s==NSEG-1) break;
    if(!FIN) *(u32x2*)ux=u;
    __syncthreads();

    if(FIN){
      u32x2 u2=*(const u32x2*)ux;
      u32x4 e0,e1,p0,p1;
      until(dl-P.dX);
      u32 Lx=LD32(hot,base+(oL^X),16); e0=LD128(hot,base+(oEX^X),16); e1=LD128(hot,base+((oEX+1024)^X),16);
      until(dl);
      p0=LD128(hot,base+32768+(oSM^X),16); p1=LD128(hot,base+32768+((oSM+256)^X),16);
      if(anysent(max(Lx,max8(e0,e1)))){
        #pragma unroll 1
        do{ asm volatile("":::"memory"); Lx=LD32(hot,base+(oL^X),16); e0=LD128(hot,base+(oEX^X),16); e1=LD128(hot,base+((oEX+1024)^X),16); } while(anysent(max(Lx,max8(e0,e1))) && --dead>0); --dead; }
      f32x4 cx0=smf64(e0,vo0,f32x4{0,0,0,0},idx), cx1=smf64(e1,vo1,f32x4{0,0,0,0},idx);
      if(anysent(max8(p0,p1))){
        #pragma unroll 1
        do{ asm volatile("":::"memory"); p0=LD128(hot,base+32768+(oSM^X),16); p1=LD128(hot,base+32768+((oSM+256)^X),16); } while(anysent(max8(p0,p1)) && --dead>0); --dead; }

      f32x4 t=BC(f32x4,p0)+(BC(f32x4,p1)-BC(float,Lx));
      float m=vmax3(vmax3(t[0],t[1],t[2]),t[3],0.f); m=maxdpp<0x128>(m);
      float a0=__builtin_amdgcn_exp2f(t[0]-m),a1=__builtin_amdgcn_exp2f(t[1]-m),a2=__builtin_amdgcn_exp2f(t[2]-m),a3=__builtin_amdgcn_exp2f(t[3]-m),f=__builtin_amdgcn_exp2f(-m);
      float sm=(a0+a1)+(a2+a3); sm+=dpp<0x128>(sm);
      float rl=__builtin_amdgcn_rcpf(sm+f), fl=f*rl; u32 ff=pk(fl,fl);
      u32x8 bz={u.x,u.y,pk(cx0[0],cx0[1]),pk(cx0[2],cx0[3]),u2.x,u2.y,pk(cx1[0],cx1[1]),pk(cx1[2],cx1[3])};
      f32x4 o=smf64(u32x4{pk(a0*rl,a1*rl),ff&dm0,pk(a2*rl,a3*rl),ff&dm1},bz,f32x4{0,0,0,0},idx);
      redw[0]=swadd(o[0],o[2]); redw[8]=swadd(o[1],o[3]);
    }
    __syncthreads();
    if(w==7){
      f32x2 a=(*(const f32x2*)redr+*(const f32x2*)(redr+576))+(*(const f32x2*)(redr+2*576)+*(const f32x2*)(redr+3*576));
      ST32(canon(pk(a[0],a[1])),hot,base+65536+((c*256+l*4)^X1),16);
    }

    if(!FIN) dl=now()+(w==7?P.dC-150:P.dC);
  }

}

#define ST128(v,r,o,aux) __builtin_amdgcn_raw_buffer_store_b128(v,r,o,0,aux)
constexpr int XROW=912, XBUF=32*XROW;
struct XStage{ u32x4 v[4]; int la[4]; };
DEV void xs_init(XStage& X,int tid){
  #pragma unroll
  for(int i=0;i<4;i++){ int p=min(tid+512*i,1791); X.la[i]=(p/56)*XROW+(p%56)*16; } }
DEV void xs_load(XStage& X,__amdgpu_buffer_rsrc_t xrs,int s,int tid){
  #pragma unroll
  for(int i=0;i<4;i++) X.v[i]=LD128(xrs,s*28672+min(tid+512*i,1791)*16,0); }
DEV void xs_put(const XStage& X,char* buf,int tid){
  #pragma unroll
  for(int i=0;i<4;i++) if(i<3||tid<256) *(u32x4*)(buf+X.la[i])=X.v[i]; }
DEV u32x4 pk8(const f32x16& a,int o){ return u32x4{pk(a[o],a[o+1]),pk(a[o+2],a[o+3]),pk(a[o+4],a[o+5]),pk(a[o+6],a[o+7])}; }

template<int SX>
DEV void kq_worker(const ChainArgs& P,char* lds,int slot,int w,int l,int tid){
  char* ws=P.ws; const int h=slot>>1,j=slot&1,m=l&31,hh=l>>5;
  auto xrs=mk(P.x,S*D*2); auto kq=mk(ws+O_KQ,NSEG*H*14*2048); auto sxx=mk(ws+O_SXX,NSEG*H*J*4096); auto flg=mk(ws+O_FLG,16384);
  XStage X; xs_init(X,tid); xs_load(X,xrs,0,tid);
  u32x4 W[28];
  if(!SX){ const char* p=ws+O_A+wf(h,224*j+32*w+sw23(m),hh);
    #pragma unroll
    for(int t=0;t<28;t++) W[t]=*(const u32x4*)(p+1024*t); }
  xs_put(X,lds,tid); xs_load(X,xrs,1,tid);
  __syncthreads();
  char* kl=lds+3*XBUF;
  int b0=0,b1=XBUF,b2=2*XBUF;
  for(int s=0;s<=NSEG;s++){
    xs_put(X,lds+b1,tid); xs_load(X,xrs,min(s+2,NSEG-1),tid);
    f32x16 c;
    #pragma unroll
    for(int i=0;i<16;i++) c[i]=0.f;
    if(!SX){ if(s<NSEG){
      const char* xp=lds+b0+m*XROW+hh*16;
      #pragma unroll
      for(int t=0;t<28;t++) c=mf32(W[t],*(const u32x4*)(xp+32*t),c);
      u32x4 k0=pk8(c,0),k1=pk8(c,8);
      vmwait();
      const u32 o=(((s*H+h)*2+(m>>4))*14+7*j+w)*1024+((m&15)+16*hh)*16;
      ST128(k0,kq,o,16); ST128(k1,kq,o+512,16);
      char* q=kl+(s&1)*14336+w*2048+l*16; *(u32x4*)q=k0; *(u32x4*)(q+1024)=k1; } }
    else if(s>0){
      const char* xp=lds+b2+m*XROW+448*j+hh*16; const char* q=kl+((s-1)&1)*14336+l*16;
      #pragma unroll
      for(int t=0;t<14;t++) c=mf32(*(const u32x4*)(q+1024*t),*(const u32x4*)(xp+32*t),c);
      vmwait();
      const u32 o=(((s-1)*H+h)*2+j)*4096+hh*512+m*16;
      #pragma unroll
      for(int q4=0;q4<4;q4++) ST128(BC(u32x4,(f32x4{c[4*q4],c[4*q4+1],c[4*q4+2],c[4*q4+3]})),sxx,o+q4*1024,16);
    }
    if(s<3||s==NSEG) vmwait();
    __syncthreads();
    if(tid==0) ST32((u32)((s<3||s==NSEG)?s+1:s),flg,slot*128,16);
    int t=b2; b2=b0; b0=b1; b1=t;
  }
}

DEV void vo_worker(const ChainArgs& P,char* lds,int c,int w,int l,int tid){
  char* ws=P.ws; const int r=l&15,g=l>>4;
  auto xrs=mk(P.x,S*D*2); auto vor=mk(ws+O_VO,NSEG*NV*8*2048); auto flg=mk(ws+O_FLG,16384);
  XStage X; xs_init(X,tid); xs_load(X,xrs,0,tid);
  u32x4 W[2][14];
  #pragma unroll
  for(int hh=0;hh<2;hh++){ const char* p=ws+O_BT+wf(2*w+hh,16*c+r,g);
    #pragma unroll
    for(int t=0;t<14;t++) W[hh][t]=*(const u32x4*)(p+2048*t); }
  xs_put(X,lds,tid); xs_load(X,xrs,1,tid);
  __syncthreads();
  for(int s=0;s<NSEG;s++){
    xs_put(X,lds+((s+1)&1)*XBUF,tid); xs_load(X,xrs,min(s+2,NSEG-1),tid);
    f32x4 a[2][2]={{{0,0,0,0},{0,0,0,0}},{{0,0,0,0},{0,0,0,0}}};
    const char* xp=lds+(s&1)*XBUF+r*XROW+g*16;
    #pragma unroll
    for(int t=0;t<14;t++){
      #pragma unroll
      for(int hf=0;hf<2;hf++){ u32x4 xa=*(const u32x4*)(xp+hf*16*XROW+64*t); a[hf][0]=mf16(xa,W[0][t],a[hf][0]); a[hf][1]=mf16(xa,W[1][t],a[hf][1]); } }
    vmwait();
    #pragma unroll
    for(int hf=0;hf<2;hf++) ST128((u32x4{pk(a[hf][0][0],a[hf][0][1]),pk(a[hf][1][0],a[hf][1][1]),pk(a[hf][0][2],a[hf][0][3]),pk(a[hf][1][2],a[hf][1][3])}),vor,(((s*NV+c)*8+w)*2+hf)*1024+l*16,16);
    if(s<3||s==NSEG-1) vmwait();
    __syncthreads();
    if(tid==0) ST32((u32)((s<3||s==NSEG-1)?s+1:s),flg,(32+c)*128,16);
  }
}

DEV float sum16(float v){ v+=dpp<0xB1>(v); v+=dpp<0x4E>(v); v+=dpp<0x141>(v); v+=dpp<0x140>(v); return v; }
DEV float gelu(float a){ float u=a*(1.5957691216057308f+0.07135481282f*a*a); return a*__builtin_amdgcn_rcpf(1.f+__builtin_amdgcn_exp2f(-1.4426950408889634f*u)); }
DEV void mlp_worker(const ChainArgs& P,char* lds,int s,int w,int l,int tid){
  char* ws=P.ws; const int m=l&31,hh=l>>5;
  char* HL=lds+XBUF+256; float* GL=(float*)(lds+XBUF);
  { const int row=4*w+(l>>4), q=l&15; const u16* xr=P.x+(size_t)(s*32+row)*D;
    float v[4][8]; float sm=0,gd=0;
    #pragma unroll
    for(int i=0;i<4;i++){ const bool ok=(i<3||q<8); const int ch=ok?q+16*i:0; u32x4 a=*(const u32x4*)(xr+ch*8), b=*(const u32x4*)(P.gw+ch*8);
      #pragma unroll
      for(int k=0;k<4;k++){ u32 av=ok?a[k]:0u; u32 bv=b[k]; v[i][2*k]=lo16(av); v[i][2*k+1]=hi16(av); sm+=v[i][2*k]+v[i][2*k+1]; gd+=v[i][2*k]*lo16(bv)+v[i][2*k+1]*hi16(bv); } }
    sm=sum16(sm); gd=sum16(gd); const float mean=sm*(1.f/D); float vr=0;
    #pragma unroll
    for(int i=0;i<4;i++){ const bool ok=(i<3||q<8);
      #pragma unroll
      for(int k=0;k<8;k++){ v[i][k]-=mean; vr+=ok?v[i][k]*v[i][k]:0.f; } }
    vr=sum16(vr); const float rs=1.f/sqrtf(vr*(1.f/D)+1e-5f);
    #pragma unroll
    for(int i=0;i<4;i++) if(i<3||q<8) *(u32x4*)(lds+row*XROW+(q+16*i)*16)=u32x4{pk(v[i][0]*rs,v[i][1]*rs),pk(v[i][2]*rs,v[i][3]*rs),pk(v[i][4]*rs,v[i][5]*rs),pk(v[i][6]*rs,v[i][7]*rs)};
    const float gt=1.f/(1.f+__expf(-gd));
    if(q==0){ GL[row]=1.f-gt; ST32(BC(u32,gt),mk(ws+O_G,S*4),(s*32+row)*4,16); }
  }
  __syncthreads();
  { const char* yp=lds+m*XROW+hh*16;
    #pragma unroll 1
    for(int ft=w;ft<56;ft+=8){
      const char* p=ws+O_W1+(size_t)ft*28672+l*16; u32x4 Wt[28];
      #pragma unroll
      for(int t=0;t<28;t++) Wt[t]=*(const u32x4*)(p+1024*t);
      u32x4 ba=*(const u32x4*)(P.b1+32*ft+8*hh), bb=*(const u32x4*)(P.b1+32*ft+16+8*hh);
      f32x16 c;
      #pragma unroll
      for(int k=0;k<4;k++){ u32 x0=ba[k],x1=bb[k]; c[2*k]=lo16(x0); c[2*k+1]=hi16(x0); c[8+2*k]=lo16(x1); c[9+2*k]=hi16(x1); }
      #pragma unroll
      for(int t=0;t<28;t++) c=mf32(Wt[t],*(const u32x4*)(yp+32*t),c);
      #pragma unroll
      for(int k=0;k<16;k++) c[k]=gelu(c[k]);
      *(u32x4*)(HL+ft*2048+l*16)=pk8(c,0); *(u32x4*)(HL+ft*2048+1024+l*16)=pk8(c,8);
    } }
  __syncthreads();
  if(w<7){
    f32x16 c[2];
    #pragma unroll
    for(int i=0;i<2;i++){
      #pragma unroll
      for(int q=0;q<4;q++){ u32x2 b=*(const u32x2*)(P.b2+64*w+32*i+8*q+4*hh); u32 x0=b.x,x1=b.y; c[i][4*q]=lo16(x0); c[i][4*q+1]=hi16(x0); c[i][4*q+2]=lo16(x1); c[i][4*q+3]=hi16(x1); } }
    #pragma unroll 1
    for(int kb=0;kb<8;kb++){
      const char* p=ws+O_W2+(size_t)(2*w*112+kb*14)*1024+l*16; u32x4 Wt[2][14];
      #pragma unroll
      for(int t=0;t<14;t++){ Wt[0][t]=*(const u32x4*)(p+1024*t); Wt[1][t]=*(const u32x4*)(p+112*1024+1024*t); }
      #pragma unroll
      for(int t=0;t<14;t++){ u32x4 hb=*(const u32x4*)(HL+(kb*14+t)*1024+l*16); c[0]=mf32(Wt[0][t],hb,c[0]); c[1]=mf32(Wt[1][t],hb,c[1]); }
    }
    const float og=GL[m]; auto bbr=mk(ws+O_BB,S*D*4);
    #pragma unroll
    for(int i=0;i<2;i++){
      #pragma unroll
      for(int q=0;q<4;q++) ST128(BC(u32x4,(f32x4{c[i][4*q]*og,c[i][4*q+1]*og,c[i][4*q+2]*og,c[i][4*q+3]*og})),bbr,((s*112+16*w+8*i+2*q+hh)*32+m)*16,16); }
  }
  vmwait(); __syncthreads();
  if(tid==0) ST32(1u,mk(ws+O_FLG,16384),(64+s)*128,16);
}

DEV float max16x(float v){ auto r=__builtin_amdgcn_permlane16_swap(BC(u32,v),BC(u32,v),false,false); u32 a=r[0],b=r[1]; return vmax3(BC(float,a),BC(float,b),BC(float,b)); }
DEV float add16x(float v){ auto r=__builtin_amdgcn_permlane16_swap(BC(u32,v),BC(u32,v),false,false); u32 a=r[0],b=r[1]; return BC(float,a)+BC(float,b); }
DEV float max32x(float v){ auto r=__builtin_amdgcn_permlane32_swap(BC(u32,v),BC(u32,v),false,false); u32 a=r[0],b=r[1]; return vmax3(BC(float,a),BC(float,b),BC(float,b)); }
DEV void out_worker(const ChainArgs& P,char* lds,int sg,int cg,int w,int l,int tid){
  char* ws=P.ws; const int r=l&15,g=l>>4; int dead=SPIN_MAX;
  auto vor=mk(ws+O_VO,NSEG*NV*8*2048); auto flg=mk(ws+O_FLG,16384); auto sxx=mk(ws+O_SXX,NSEG*H*J*4096);
  auto sxm=mk(ws+O_SXM,NSEG*H*J*1024); auto upr=mk(ws+O_UP,NSEG*NV*8*512); auto bbr=mk(ws+O_BB,S*D*4); auto gr=mk(ws+O_G,S*4); auto outr=mk(P.out,S*D*2);
  #pragma unroll 1
  for(int s=sg;s<NSEG;s+=8){
    waitprog(flg,l<4?4*w+l:(l<8?32+4*cg+(l-4):64+s),l<4?s+2:(l<8?s+1:1),dead,16);
    f32x4 sv[2][2][2]; u32x4 vv[4][2];
    #pragma unroll
    for(int hh=0;hh<2;hh++){ const u32 o=((s*H+2*w+hh)*2)*4096+g*512+r*16;
      sv[hh][0][0]=BC(f32x4,LD128(sxx,o,16))+BC(f32x4,LD128(sxx,o+4096,16));
      sv[hh][1][0]=BC(f32x4,LD128(sxx,o+256,16))+BC(f32x4,LD128(sxx,o+4096+256,16));
      sv[hh][1][1]=BC(f32x4,LD128(sxx,o+2048+256,16))+BC(f32x4,LD128(sxx,o+4096+2048+256,16)); }
    #pragma unroll
    for(int ct=0;ct<4;ct++){ const u32 o=((s*NV+4*cg+ct)*8+w)*2048+l*16; vv[ct][0]=LD128(vor,o,16); vv[ct][1]=LD128(vor,o+1024,16); }
    const int ort=w>>2, oct=w&3;
    const f32x4 bb=BC(f32x4,LD128(bbr,((s*112+16*cg+4*oct+g)*32+16*ort+r)*16,16));
    const float gt=BC(float,__builtin_amdgcn_raw_buffer_load_b32(gr,(s*32+16*ort+r)*4,0,16));
    f32x4 tmp[2][2][4]; float mx[2],lx[2];
    #pragma unroll
    for(int hh=0;hh<2;hh++){
      #pragma unroll
      for(int rt=0;rt<2;rt++){
        f32x4 a=sv[hh][rt][0], b=sv[hh][rt][rt];
        #pragma unroll
        for(int i=0;i<4;i++) b[i]=(4*g+i<=r)?b[i]:-3.0e38f;
        float mm=vmax3(vmax3(b[0],b[1],b[2]),b[3],b[3]); if(rt) mm=vmax3(vmax3(a[0],a[1],mm),a[2],a[3]);
        mm=max32x(max16x(mm)); const float mc=mm*CSC; float sum=0;
        #pragma unroll
        for(int i=0;i<4;i++){ b[i]=(4*g+i<=r)?__builtin_amdgcn_exp2f(b[i]*CSC-mc):0.f; sum+=b[i]; if(rt){ a[i]=__builtin_amdgcn_exp2f(a[i]*CSC-mc); sum+=a[i]; } }
        sum=add16x(sum); sum=swadd(sum,sum);
        if(hh==0||g>=2){ mx[rt]=mc; lx[rt]=sum; }
        const u32x4 e= rt? u32x4{pk(a[0],a[1]),pk(a[2],a[3]),pk(b[0],b[1]),pk(b[2],b[3])} : u32x4{pk(b[0],b[1]),pk(b[2],b[3]),0u,0u};
        #pragma unroll
        for(int ct=0;ct<4;ct++) tmp[hh][rt][ct]=mf16(u32x4{vv[ct][0][hh],vv[ct][0][2+hh],vv[ct][1][hh],vv[ct][1][2+hh]},e,f32x4{0,0,0,0});
      } }
    const u32 om=((s*H+2*w+(g>>1))*2)*1024+(g&1)*256+r*16, ou=((s*NV+4*cg)*8+w)*512+l*8;
    { u32 v;
      #pragma unroll 1
      while(true){ asm volatile("":::"memory"); v=__builtin_amdgcn_raw_buffer_load_b32(sxm,((s*H+2*w)*2)*1024,0,16); if(__builtin_amdgcn_readfirstlane(v)!=SENT || --dead<=0) break; nap(4); } }
    u32x4 q[2][2]; u32x2 uu[4];
    #pragma unroll 1
    while(true){ asm volatile("":::"memory"); u32 mxs=0;
      #pragma unroll
      for(int rt=0;rt<2;rt++){ q[rt][0]=LD128(sxm,om+rt*512,16); q[rt][1]=LD128(sxm,om+1024+rt*512,16); }
      #pragma unroll
      for(int ct=0;ct<4;ct++) uu[ct]=LD64(upr,ou+ct*4096,16);
      #pragma unroll
      for(int rt=0;rt<2;rt++){ mxs=umax3(umax3(mxs,q[rt][0].x,q[rt][0].y),q[rt][0].z,q[rt][0].w); mxs=umax3(umax3(mxs,q[rt][1].x,q[rt][1].y),q[rt][1].z,q[rt][1].w); }
      #pragma unroll
      for(int ct=0;ct<4;ct++) mxs=umax3(mxs,uu[ct].x,uu[ct].y);
      if(!anysent(mxs) || --dead<=0) break; nap(2); }
    #pragma unroll
    for(int rt=0;rt<2;rt++){
      f32x4 t=(BC(f32x4,q[rt][0])+BC(f32x4,q[rt][1]))*CSC;
      float mm=max16x(vmax3(vmax3(t[0],t[1],t[2]),t[3],mx[rt]));
      float a0=__builtin_amdgcn_exp2f(t[0]-mm),a1=__builtin_amdgcn_exp2f(t[1]-mm),a2=__builtin_amdgcn_exp2f(t[2]-mm),a3=__builtin_amdgcn_exp2f(t[3]-mm),f=__builtin_amdgcn_exp2f(mx[rt]-mm);
      const float rl=__builtin_amdgcn_rcpf(add16x((a0+a1)+(a2+a3))+f*lx[rt]), sc=f*rl;
      auto sp=__builtin_amdgcn_permlane32_swap(BC(u32,sc),BC(u32,sc),false,false); u32 s0=sp[0],s1=sp[1]; const float sA=BC(float,s0),sB=BC(float,s1);
      const u32x2 pm={pk(a0*rl,a1*rl),pk(a2*rl,a3*rl)};
      #pragma unroll
      for(int ct=0;ct<4;ct++) *(f32x4*)(lds+((w*8+rt*4+ct)*64+l)*16)=mf16k(uu[ct],pm,tmp[0][rt][ct]*sA+tmp[1][rt][ct]*sB);
    }
    __syncthreads();
    f32x4 o=*(const f32x4*)(lds+(w*64+l)*16);
    #pragma unroll
    for(int k=1;k<8;k++) o+=*(const f32x4*)(lds+((k*8+w)*64+l)*16);
    o=o*gt+bb;
    ST64((u32x2{pk(o[0],o[1]),pk(o[2],o[3])}),outr,((s*32+16*ort+r)*D+64*cg+16*oct+4*g)*2,0);
    __syncthreads();
  }
}

#define XS 3
constexpr int XV=XS^2, XK=XS^1, XO=XS^3;
constexpr int LDS2=XBUF+256+114688;
__global__ __launch_bounds__(512,2) void k_main(ChainArgs P){
  extern __shared__ __attribute__((aligned(16))) char lds[];
  const int rot=*(const int*)(P.ws+O_XOR)&7;
  const int b=blockIdx.x, xcd=(b+rot)&7, slot=b>>3, cls=(XS-rot)&7;
  const int tid=threadIdx.x, l=tid&63, w=__builtin_amdgcn_readfirstlane(tid>>6);
  if(xcd==XS || xcd==XV){
    const int up=xcd==XV;
    if(slot<16){ const int sl=up*16+slot;
      if(w==0) score_loop<0>(P,lds,sl,w,l,cls);
      else if(w<3) score_loop<1>(P,lds,sl,w,l,cls);
      else if(w<7) score_loop<2>(P,lds,sl,w,l,cls);
      else if((sl&1)==0) score_loop<3>(P,lds,sl,w,l,cls);
      else score_loop<4>(P,lds,sl,w,l,cls);
    } else if(slot<30){ const int c=up*14+slot-16; if(w<4) value_loop<1>(P,lds,c,w,l,cls); else value_loop<0>(P,lds,c,w,l,cls); }
  }
  else if(xcd==XK){ if(w<7) kq_worker<0>(P,lds,slot,w,l,tid); else kq_worker<1>(P,lds,slot,w,l,tid); }
  else if(xcd==XO){ if(slot<NV) vo_worker(P,lds,slot,w,l,tid); }
  else if(((xcd^XS)&6)==4) mlp_worker(P,lds,slot*2+(xcd&1),w,l,tid);
  else if(slot<28) out_worker(P,lds,(slot/7)*2+(xcd&1),slot%7,w,l,tid);
}

struct PreArgs{ const u16* mem0; const u16* Wq; const u16* Wk; const u16* Wv; const u16* Wo; const u16* w1; const u16* w2; char* ws; };
template<int WH>
DEV void ab_gemm(const PreArgs& P,char* lds,int h,int mb,int w,int l,int tid){
  const int m=l&31,hh=l>>5; u32x4 st[4][8];
  const u16* Np=(WH?P.Wv:P.Wk)+(size_t)(tid>>3)*4096+h*256+(tid&7)*8;
  const u16* Mp= WH? P.Wo+(size_t)(h*256+(tid>>3))*D+64*mb+(tid&7)*8 : P.Wq+(size_t)(64*mb+(tid>>3))*4096+h*256+(tid&7)*8;
  #pragma unroll
  for(int q=0;q<4;q++){
    #pragma unroll
    for(int i=0;i<7;i++) st[q][i]=*(const u32x4*)(Np+(size_t)i*64*4096+64*q);
    st[q][7]=*(const u32x4*)(Mp+(WH?64*D:64)*q); }
  const int la=(tid>>3)*128+(((tid&7)^((tid>>4)&7))*16), lam=WH?57344+tid*16:57344+la;
  f32x16 c[2][2];
  #pragma unroll
  for(int i=0;i<16;i++){ c[0][0][i]=0;c[0][1][i]=0;c[1][0][i]=0;c[1][1][i]=0; }
  const int ra=sw23(m);
  #pragma unroll
  for(int hk=0;hk<2;hk++){
    if(hk) __syncthreads();
    #pragma unroll
    for(int q=0;q<2;q++){
      #pragma unroll
      for(int i=0;i<7;i++) *(u32x4*)(lds+q*65536+i*8192+la)=st[2*hk+q][i];
      *(u32x4*)(lds+q*65536+lam)=st[2*hk+q][7]; }
    __syncthreads();
    if(w<7){
      #pragma unroll
      for(int q=0;q<2;q++){
        #pragma unroll
        for(int t=0;t<4;t++){ u32x4 a[2],b[2];
          #pragma unroll
          for(int i=0;i<2;i++){
            a[i]=*(const u32x4*)(lds+q*65536+(64*w+32*i+ra)*128+(((2*t+hh)^((ra>>1)&7))*16));
            if(WH){ const char* p=lds+q*65536+57344+(16*t+8*hh+((l&15)>>2))*128+(32*i+16*((l>>4)&1)+4*(l&3))*2; u32x2 x0=trread(p),x1=trread(p+512); b[i]=u32x4{x0.x,x0.y,x1.x,x1.y}; }
            else b[i]=*(const u32x4*)(lds+q*65536+57344+(32*i+m)*128+(((2*t+hh)^((m>>1)&7))*16)); }
          c[0][0]=mf32(a[0],b[0],c[0][0]); c[0][1]=mf32(a[0],b[1],c[0][1]); c[1][0]=mf32(a[1],b[0],c[1][0]); c[1][1]=mf32(a[1],b[1],c[1][1]);
        } } } }
  if(w<7){ char* o=P.ws+(WH?O_BT:O_A);
    #pragma unroll
    for(int ti=0;ti<2;ti++){
      #pragma unroll
      for(int tj=0;tj<2;tj++){ char* p=o+wf(h,64*mb+32*tj+m,8*w+4*ti+hh); *(u32x4*)p=pk8(c[ti][tj],0); *(u32x4*)(p+1024)=pk8(c[ti][tj],8); } } }
}
__global__ __launch_bounds__(512,2) void k_pre(PreArgs P){
  extern __shared__ __attribute__((aligned(16))) char lds[];
  const int b=blockIdx.x, xcd=b&7, slot=b>>3;
  const int tid=threadIdx.x, l=tid&63, w=__builtin_amdgcn_readfirstlane(tid>>6);
  char* ws=P.ws; const u32x4 sent={SENT,SENT,SENT,SENT}; auto hot=mk(ws+O_HOT,66*65536);
  if(slot<28){ const int gm=xcd*4+slot/7; if(gm&1) ab_gemm<1>(P,lds,gm>>1,slot%7,w,l,tid); else ab_gemm<0>(P,lds,gm>>1,slot%7,w,l,tid); }
  else {
    const int mi=(slot-28)*8+xcd;
    if(slot==28){
      u32 best=~0u,bj=0;
      for(int rep=0;rep<4;rep++){ u32* p=(u32*)(ws+O_HOT+(size_t)w*524288+32768+xcd*512+rep*128); u32 v=SENT;
        asm volatile("global_store_dword %0, %4, off sc1\n global_store_dword %1, %4, off sc1\n global_store_dword %2, %4, off sc1\n global_store_dword %3, %4, off sc1\n s_waitcnt vmcnt(0)"::"v"(p),"v"(p+2048),"v"(p+4096),"v"(p+6144),"v"(v):"memory");
        for(int j=0;j<4;j++){ u32 t0=now(); asm volatile("global_load_dword %0, %1, off sc1\n s_waitcnt vmcnt(0)":"=v"(v):"v"(p+2048*j):"memory"); u32 t=now()-t0; if(t<best){ best=t; bj=j; } } }
      if(l==0) ((u32*)(ws+O_XOR))[16+xcd*8+w]=bj;
    }
    if(b==224 && tid==0){ u32 v; asm volatile("s_getreg_b32 %0, hwreg(HW_REG_XCC_ID)":"=s"(v)); *(u32*)(ws+O_XOR)=v&7; }
    if(mi==1){ for(int k=tid;k<1792;k+=512){ int c=k>>6, hi=(k>>5)&1, i=(k>>2)&7, q=k&3; u32 v=canon(*(const u32*)(P.mem0+i*D+16*c+4*q+2*hi)); ST32(v,hot,64*65536+k*4,16); }
      if(tid<128) ST32(0u,mk(ws+O_FLG,16384),tid*128,16); }
    for(int id=mi*512+tid;id<2*100352;id+=32*512){ const bool two=id>=100352; const int k=two?id-100352:id, ln=k&63, mm=ln&31, h2=ln>>5, fr=k>>6;
      const u16* p; int str;
      if(!two){ p=P.w1+(size_t)(16*(fr%28)+8*h2)*F+32*(fr/28)+sw23(mm); str=F; } else { p=P.w2+(size_t)(16*(fr%112)+8*h2)*D+32*(fr/112)+mm; str=D; }
      u32 e[8];
      #pragma unroll
      for(int j=0;j<8;j++) e[j]=p[j*str];
      *(u32x4*)(ws+O_W1+(size_t)id*16)=u32x4{e[0]|(e[1]<<16),e[2]|(e[3]<<16),e[4]|(e[5]<<16),e[6]|(e[7]<<16)}; }
  }
  for(u32 i=b*512+tid;i<64*4096;i+=256*512) ST128(sent,hot,i*16,16);
  for(u32 i=b*512+tid;i<(O_SXX-O_UP)/16;i+=256*512) ST128(sent,mk(ws+O_UP,(u32)(O_SXX-O_UP)),i*16,16);
}
extern "C" void kernel_launch(void* const* d_in,const int* in_sizes,int n_in,void* d_out,int out_size,void* d_ws,size_t ws_size,hipStream_t stream){
  char* ws=(char*)(((size_t)d_ws+2097151)&~(size_t)2097151);
  (void)hipFuncSetAttribute((const void*)k_pre,hipFuncAttributeMaxDynamicSharedMemorySize,131072);
  (void)hipFuncSetAttribute((const void*)k_main,hipFuncAttributeMaxDynamicSharedMemorySize,LDS2);
  PreArgs Q{(const u16*)d_in[1],(const u16*)d_in[2],(const u16*)d_in[3],(const u16*)d_in[4],(const u16*)d_in[5],(const u16*)d_in[8],(const u16*)d_in[10],ws};
  k_pre<<<256,512,131072,stream>>>(Q);
  ChainArgs P{(const u16*)d_in[0],ws,(const u16*)d_in[9],(const u16*)d_in[11],(const u16*)d_in[12],(u16*)d_out,2200,1400,600,2500,300,0,0,0};
  k_main<<<256,512,LDS2,stream>>>(P);
}
```

```cpp
#include <hip/hip_runtime.h>
#include <hip/hip_bf16.h>
typedef unsigned u32; typedef unsigned long long u64; typedef unsigned short u16;
typedef u32 u32x2 __attribute__((ext_vector_type(2)));
typedef u32 u32x4 __attribute__((ext_vector_type(4)));
typedef u32 u32x8 __attribute__((ext_vector_type(8)));
typedef float f32x2 __attribute__((ext_vector_type(2)));
typedef float f32x4 __attribute__((ext_vector_type(4)));
typedef __bf16 bf16x2 __attribute__((ext_vector_type(2)));
typedef __bf16 bf16x8 __attribute__((ext_vector_type(8)));
typedef __bf16 bf16x16 __attribute__((ext_vector_type(16)));
typedef float f32x16 __attribute__((ext_vector_type(16)));
typedef short s16x4 __attribute__((ext_vector_type(4)));
typedef short s16x8 __attribute__((ext_vector_type(8)));
#define DEV __device__ __forceinline__
template<class T,class U> __device__ __forceinline__ T bcf(U x){ return __builtin_bit_cast(T,x); }
#define BC(T,x) bcf<T>(x)

constexpr int D=448, H=16, DH=256, S=2048, NSEG=64, F=1792, J=2, NV=28;
constexpr float CSC = 1.4426950408889634f/16.0f;
constexpr size_t O_HOT=0;
constexpr size_t O_XOR=O_HOT+(size_t)66*65536;
constexpr size_t O_FLG=O_XOR+32768;
constexpr size_t O_A  =O_XOR+65536;
constexpr size_t O_BT =O_A+(size_t)H*D*D*2;
constexpr size_t O_KQ =O_BT+(size_t)H*D*D*2;
constexpr size_t O_VO =O_KQ+(size_t)NSEG*H*14*2048;
constexpr size_t O_UP =O_VO+(size_t)NSEG*NV*8*2048;
constexpr size_t O_SXM=O_UP+(size_t)NSEG*NV*8*512;
constexpr size_t O_SXX=O_SXM+(size_t)NSEG*H*J*1024;
constexpr size_t O_BB =O_SXX+(size_t)NSEG*H*J*4096;
constexpr size_t O_G  =O_BB+(size_t)S*D*4;
constexpr size_t O_W1 =O_G+(size_t)S*4;
constexpr size_t O_W2 =O_W1+(size_t)D*F*2;
constexpr size_t O_END=O_W2+(size_t)D*F*2;

DEV float bf2f(u16 v){ return BC(float,(u32)v<<16); }
DEV u16 f2bf(float f){ __bf16 b=(__bf16)f; return BC(u16,b); }
DEV u32 pk(float a,float b){ bf16x2 v={(__bf16)a,(__bf16)b}; return BC(u32,v); }
DEV __amdgpu_buffer_rsrc_t mk(const void* p, u32 bytes){ return __builtin_amdgcn_make_buffer_rsrc((void*)p,0,bytes,0x00020000); }
DEV u32 now(){ u64 t; asm volatile("s_memtime %0\n s_waitcnt lgkmcnt(0)":"=s"(t)::"memory"); return (u32)t; }
DEV f32x4 smf64(u32x4 a,u32x8 b,f32x4 c,int idx){ return __builtin_amdgcn_smfmac_f32_16x16x64_bf16(BC(bf16x8,a),BC(bf16x16,b),c,idx,0,0); }
DEV f32x4 smf32(u32x2 a,u32x4 b,f32x4 c,int idx){ return __builtin_amdgcn_smfmac_f32_16x16x32_bf16(BC(s16x4,a),BC(s16x8,b),c,idx,0,0); }
DEV float swadd(float a,float b){ auto r=__builtin_amdgcn_permlane32_swap(BC(u32,a),BC(u32,b),false,false); return BC(float,r[0])+BC(float,r[1]); }
template<int C> DEV float dpp(float x){ return BC(float,__builtin_amdgcn_update_dpp(0,BC(int,x),C,0xF,0xF,true)); }
DEV float vmax3(float a,float b,float c){ float r; asm("v_max3_f32 %0,%1,%2,%3":"=v"(r):"v"(a),"v"(b),"v"(c)); return r; }
template<int C> DEV float maxdpp(float x){ float y=dpp<C>(x); return vmax3(x,y,y); }
DEV u32x8 cat(u32x4 a,u32x4 b){ return __builtin_shufflevector(a,b,0,1,2,3,4,5,6,7); }
DEV u32 canon(u32 x){ return x>0xFFFFFFFEu?0xFFFFFFFEu:x; }
DEV u32x2 trread(const char* p){ return BC(u32x2,__builtin_amdgcn_ds_read_tr16_b64_v4i16((__attribute__((address_space(3))) s16x4*)p)); }
constexpr int SPIN_MAX=400000;
#define SENT 0xFFFFFFFFu
DEV u32 umax3(u32 a,u32 b,u32 c){ return max(max(a,b),c); }
DEV bool anysent(u32 m){ return __builtin_amdgcn_ballot_w64(m==SENT)!=0; }
DEV void nap(int n){ for(int i=0;i<n;i++) __builtin_amdgcn_s_sleep(1); }
DEV void until(u32 t){ int d; while((d=(int)(t-now()))>0){ if(d>160) __builtin_amdgcn_s_sleep(1); } }
DEV u32 max8(u32x4 a,u32x4 b){ return max(umax3(umax3(a.x,a.y,a.z),a.w,b.x),umax3(b.y,b.z,b.w)); }
DEV u32x4 poll16(__amdgpu_buffer_rsrc_t r,u32 off,int& budget){
  u32x4 v;
  #pragma unroll 1
  do { asm volatile("":::"memory"); v=__builtin_amdgcn_raw_buffer_load_b128(r,off,0,16);
  } while(anysent(max(umax3(v.x,v.y,v.z),v.w)) && --budget>0);
  return v;
}
#define CALT 320
#define CALN 4
DEV u32 loadX(const char* ws,int cls){ const int* xr=(const int*)(ws+O_XOR)+16; const int l=threadIdx.x&63; int t0=0;
  for(int q=0;q<4;q++) t0+=xr[((cls+4*(l>>5)+q)&7)*32+(l&31)];
  int t=(((l>>3)^(l>>5))&1)?-t0:t0;
  t+=__shfl_xor(t,8); t+=__shfl_xor(t,16); t+=__shfl_xor(t,32);
  int p=t+__shfl_xor(t,1); p+=__shfl_xor(p,2);
  const u32 m=(u32)__builtin_amdgcn_ballot_w64((t>960||t<-960)?t>0:p>0);
  u32 X=0; for(int i=0;i<8;i++)X|=((m>>i)&1u)<<(2*i); return X; }
#define XM(s) (((XX>>(((s)>>3)*2))&3u)<<13)

DEV f32x16 mf32(u32x4 a,u32x4 b,f32x16 c){ return __builtin_amdgcn_mfma_f32_32x32x16_bf16(BC(bf16x8,a),BC(bf16x8,b),c,0,0,0); }
DEV f32x4 mf16(u32x4 a,u32x4 b,f32x4 c){ return __builtin_amdgcn_mfma_f32_16x16x32_bf16(BC(bf16x8,a),BC(bf16x8,b),c,0,0,0); }
DEV f32x4 mf16k(u32x2 a,u32x2 b,f32x4 c){ return __builtin_amdgcn_mfma_f32_16x16x16bf16_1k(BC(s16x4,a),BC(s16x4,b),c,0,0,0); }
DEV int sw23(int m){ return (m&19)|((m&4)<<1)|((m&8)>>1); }
DEV u32 wf(int h,int row,int kc){ return (u32)(((h*14+(row>>5))*56+kc)*512+(row&31)*16); }
DEV void vmwait(){ asm volatile("":::"memory"); __builtin_amdgcn_s_waitcnt(0x0F70); asm volatile("":::"memory"); }
DEV float lo16(u32 v){ return BC(float,v<<16); }
DEV float hi16(u32 v){ return BC(float,v&0xFFFF0000u); }
DEV int waitprog(__amdgpu_buffer_rsrc_t fl,int idx,int need,int& budget,int slp){
  int v;
  #pragma unroll 1
  while(true){ asm volatile("":::"memory"); v=(int)__builtin_amdgcn_raw_buffer_load_b32(fl,idx*128,0,16);
    if(__builtin_amdgcn_ballot_w64(v<need)==0 || --budget<=0) break; nap(slp); }
  return v;
}

struct ChainArgs{ const u16* x; char* ws; const u16* b1; const u16* b2; const u16* gw; const u16* w1; const u16* w2; u16* out; int dA; int dB; int dC; int dD; int dE; int dX; int dG; int ts; };
constexpr int L_MEM=0, L_TT=7168, L_SX=L_TT+2*2112+64, L_L=L_SX+1024, L_RED=7168, L_U=L_RED+4096;

#define LD128(r,o,aux) __builtin_amdgcn_raw_buffer_load_b128(r,o,0,aux)
#define LD32(r,o,aux) __builtin_amdgcn_raw_buffer_load_b32(r,o,0,aux)
#define LD64(r,o,aux) __builtin_amdgcn_raw_buffer_load_b64(r,o,0,aux)
#define ST64(v,r,o,aux) __builtin_amdgcn_raw_buffer_store_b64(v,r,o,0,aux)
#define ST32(v,r,o,aux) __builtin_amdgcn_raw_buffer_store_b32(v,r,o,0,aux)

template<int ROLE>
DEV void score_loop(const ChainArgs& P,char* lds,int slot,int w,int l,int cls){
  const int r=l&15,g=l>>4; char* ws=P.ws;
  const int h=slot>>1, j=slot&1, n0=224*j;
  auto hot=mk(ws+O_HOT,66*65536);
  const u32 XX=loadX(ws,cls);
  const int idx=(r<8)?0x4444:0xEEEE;
  int dead=SPIN_MAX;
  u32x8 W[2][7];
  auto kq=mk(ws+O_KQ,NSEG*H*14*2048); auto flg=mk(ws+O_FLG,16384); int have=0;
  if(ROLE<3){
    #pragma unroll
    for(int tt=0;tt<2;tt++){ const char* p=ws+O_A+wf(h,n0+32*w+16*tt+r,g);
      #pragma unroll
      for(int t=0;t<7;t++) W[tt][t]=cat(*(const u32x4*)(p+4096*t),*(const u32x4*)(p+4096*t+2048)); }
  } else if(ROLE==3){
    { int v=waitprog(flg,2*h+(l&1),1,dead,1); have=min(__builtin_amdgcn_readlane(v,0),__builtin_amdgcn_readlane(v,1)); }
    #pragma unroll
    for(int k=0;k<14;k++){ u32 o=(u32)h*14*2048+k*2048+l*16; W[k/7][k%7]=cat(LD128(kq,o,16),LD128(kq,o+1024,16)); }
    __builtin_amdgcn_s_setprio(3);
  }
  if(ROLE==0){ *(u32x2*)(lds+L_TT+(l>>5)*2112+(224+(l&31))*8)=u32x2{0,0}; }
  if(ROLE==0) __builtin_amdgcn_s_setprio(2);
  u32x4 xb[7];
  auto xrs=mk(P.x,S*D*2);
  const u32 xo=(u32)((16*(w-1)+r)*D+n0+8*g)*2;
  if(ROLE==1){
    #pragma unroll
    for(int k=0;k<7;k++) xb[k]=LD128(xrs,xo+64*k,0);
  }
  const int rr=r>>2, hp=(l>>1)&1, q1=l&1;
  const char* trp=lds+L_TT+q1*2112+(32*(g>>1)+16*(g&1)+(ROLE==0?4*hp+rr:2*hp+(rr&1)+4*(rr>>1)))*8;
  auto sxm=mk(ws+O_SXM,NSEG*H*J*1024);
  __builtin_amdgcn_s_waitcnt(0x0F70);

  u32 dl=now();
  for(int s=0;s<NSEG;s++){
    const u32 X=XM(s); const u32 base=s*65536u;
    if(ROLE<3){ until(dl); u32x4 v=poll16(hot,(s?base:64*65536u)+((w*1024+l*16)^(s?X:0u)),dead); if(s) {  }  *(u32x4*)(lds+L_MEM+w*1024+l*16)=v; }
    __syncthreads();
    if(ROLE==4){  } else {  }
    float v0,v1,v2,v3,mc; u32x2 mb[14]; u32x4 am[7];
    if(ROLE<4){
      f32x4 c0={0,0,0,0},c1={0,0,0,0};
      #pragma unroll
      for(int t=0;t<7;t++) am[t]=*(const u32x4*)(lds+L_MEM+t*1024+l*16);
      __builtin_amdgcn_sched_barrier(0);
      #pragma unroll
      for(int t=0;t<7;t++){ c0=smf64(am[t],W[0][t],c0,idx); c1=smf64(am[t],W[1][t],c1,idx); }
      if(ROLE==0){
        #pragma unroll
        for(int k=0;k<7;k++){ const char* p=lds+L_MEM+(14*j+2*k+(g>>1))*256+(l&7)*16+(g&1)*8; mb[2*k]=*(const u32x2*)p; mb[2*k+1]=*(const u32x2*)(p+128); }
      }
      v0=swadd(c0[0],c1[0]);v1=swadd(c0[1],c1[1]);v2=swadd(c0[2],c1[2]);v3=swadd(c0[3],c1[3]);
      asm volatile("":"+v"(v0),"+v"(v3));
      if(ROLE<3) *(u32x2*)(lds+L_TT+(g&1)*2112+(32*w+16*(l>>5)+r)*8)=u32x2{pk(v0,v1),pk(v2,v3)};
      if(ROLE==3){
        float* sx=(float*)(lds+L_SX)+(4*(g&1))*32+16*(l>>5)+r;
        sx[0]=v0; sx[32]=v1; sx[64]=v2; sx[96]=v3;
        __builtin_amdgcn_wave_barrier();
        f32x4 z=*(const f32x4*)(lds+L_SX+l*16);
        float m=vmax3(vmax3(z[0],z[1],z[2]),z[3],z[3]);
        m=maxdpp<0xB1>(m); m=maxdpp<0x4E>(m); m=maxdpp<0x141>(m);
        mc=m*CSC;
        v0=__builtin_amdgcn_exp2f(z[0]*CSC-mc);v1=__builtin_amdgcn_exp2f(z[1]*CSC-mc);v2=__builtin_amdgcn_exp2f(z[2]*CSC-mc);v3=__builtin_amdgcn_exp2f(z[3]*CSC-mc);
        asm volatile("":"+v"(v0),"+v"(v3));
      }
    }
    __syncthreads();
    if(ROLE<4){  }
    if(ROLE==0){
      if(s<NSEG-1){
        f32x4 c={0,0,0,0},c2={0,0,0,0}; u32x2 ta[8];
        #pragma unroll
        for(int u=0;u<8;u++) ta[u]=trread(trp+(u>>1)*512+(u&1)*64);
        __builtin_amdgcn_sched_barrier(0);
        #pragma unroll
        for(int u=0;u<4;u++){
          u32x2 a0=ta[2*u], a1=ta[2*u+1];
          u32x4 f0={mb[4*u].x,mb[4*u].y,mb[4*u+1].x,mb[4*u+1].y};
          u32x4 f1= u<3? u32x4{mb[(4*u+2)%14].x,mb[(4*u+2)%14].y,mb[(4*u+3)%14].x,mb[(4*u+3)%14].y} : u32x4{0,0,0,0};
          if(u&1) c2=smf64(u32x4{a0.x,a0.y,a1.x,a1.y},cat(f0,f1),c2,idx); else c=smf64(u32x4{a0.x,a0.y,a1.x,a1.y},cat(f0,f1),c,idx);
        }
        __builtin_amdgcn_sched_barrier(0);
        c+=c2; float sa=swadd(c[0],c[2])*CSC, sb=swadd(c[1],c[3])*CSC;
        if(r<8) ST64((u32x2{canon(BC(u32,sa)),canon(BC(u32,sb))}),hot,base+32768+(((h*J+j)*256+(g>>1)*128+r*16+(g&1)*8)^X),16);
      }

      dl=now()+P.dA;
    } else if(ROLE==2){ dl=now()+P.dD;
    } else if(ROLE==1){
      dl=now(); until(dl+P.dE); dl+=P.dD;
      f32x4 c={0,0,0,0};
      #pragma unroll
      for(int u=0;u<4;u++){
        u32x2 a0=trread(trp+u*512), a1=trread(trp+u*512+64);
        c=smf64(u32x4{a0.x,a0.y,a1.x,a1.y},cat(xb[2*u],u<3?xb[(2*u+1)%7]:u32x4{0,0,0,0}),c,idx);
      }
      float sa=swadd(c[0],c[2]), sb=swadd(c[1],c[3]);
      ST64((u32x2{canon(BC(u32,sa)),canon(BC(u32,sb))}),sxm,((s*H+h)*J+j)*1024+(w-1)*512+(g&1)*256+r*16+(g>>1)*8,16);
      const u32 xn=xo+min(s+1,NSEG-1)*32*D*2;
      #pragma unroll
      for(int k=0;k<7;k++) xb[k]=LD128(xrs,xn+64*k,0);
    } else if(ROLE==3 && s<NSEG-1){
      float sm=(v0+v1)+(v2+v3);
      sm+=dpp<0xB1>(sm); sm+=dpp<0x4E>(sm); sm+=dpp<0x141>(sm);
      float rl=__builtin_amdgcn_rcpf(sm);
      ST64((u32x2{canon(pk(v0*rl,v1*rl)),canon(pk(v2*rl,v3*rl))}),hot,base+((16384+h*512+l*8)^X),16);
      if((l&7)==0) ST32(canon(BC(u32,mc+__builtin_amdgcn_logf(sm))),hot,base+((7168+h*32+(l>>3)*4)^X),16);

      until(now()+P.dE);
      if(have<s+2){ int v=waitprog(flg,2*h+(l&1),s+2,dead,1); have=min(__builtin_amdgcn_readlane(v,0),__builtin_amdgcn_readlane(v,1)); }
      const u32 o=(u32)((s+1)*H+h)*14*2048+l*16;
      #pragma unroll
      for(int k=0;k<14;k++) W[k/7][k%7]=cat(LD128(kq,o+k*2048,16),LD128(kq,o+k*2048+1024,16));
    }
  }

}

template<int FIN>
DEV void value_loop(const ChainArgs& P,char* lds,int c,int w,int l,int cls){
  const int r=l&15,g=l>>4; char* ws=P.ws;
  auto hot=mk(ws+O_HOT,66*65536);
  const u32 XX=loadX(ws,cls);
  const int idx=(r<8)?0x4444:0xEEEE;
  int dead=SPIN_MAX;
  const int pr=FIN?2*w:2*(w-4)+1;
  u32x8 W[2][7];
  #pragma unroll
  for(int hh=0;hh<2;hh++){ const char* p=ws+O_BT+wf(2*pr+hh,16*c+r,g);
    #pragma unroll
    for(int t=0;t<7;t++) W[hh][t]=cat(*(const u32x4*)(p+4096*t),*(const u32x4*)(p+4096*t+2048)); }
  auto flg=mk(ws+O_FLG,16384); int have=0;
  auto vor=mk(ws+O_VO,NSEG*NV*8*2048);
  auto upr=mk(ws+O_UP,NSEG*NV*8*512);
  u32 voo=(c*8+pr)*2048+l*16;
  u32x8 vo0,vo1;
  if(FIN) __builtin_amdgcn_s_setprio(2);
  const int i=l&7, hi=(l>>3)&1;
  const u32 dm0=(i==2*hi?0xFFFFu:0u)|(i==2*hi+1?0xFFFF0000u:0u), dm1=(i==4+2*hi?0xFFFFu:0u)|(i==5+2*hi?0xFFFF0000u:0u);
  const u32 oEX=16384+(4*w+hi)*512+i*64+g*16, oSM=(4*w+g)*J*256+hi*128+i*16, oL=7168+(4*w+g)*32+i*4;
  float* redw=(float*)(lds+L_RED)+w*144+((r>>1)&1)*72+(4*(g&1)+2*(l>>5))*8+(r>>2)*2+(r&1);
  const char* redr=lds+L_RED+((l>>5)*72+(l&31)*2)*4;
  char* ux=lds+L_U+(w&3)*512+l*8;
  __builtin_amdgcn_s_waitcnt(0x0F70);

  u32 dl=now();
  for(int s=0;s<NSEG;s++){
    const u32 X=XM(s), X1=XM(s+1); const u32 base=s*65536u;
    if(!FIN){ until(dl); const u32 mb=s?base:64*65536u, mx=s?X:0u; const u32 o0=mb+(((w-4)*2048+l*16)^mx), o1=mb+((min((w-4)*2048+1024,6144)+l*16)^mx); u32x4 v0,v1;
      #pragma unroll 1
      do{ asm volatile("":::"memory"); v0=LD128(hot,o0,16); v1=LD128(hot,o1,16);
      } while(anysent(max(umax3(umax3(v0.x,v0.y,v0.z),v0.w,v1.x),umax3(v1.y,v1.z,v1.w))) && --dead>0);

      *(u32x4*)(lds+L_MEM+(w-4)*2048+l*16)=v0; if(w<7) *(u32x4*)(lds+L_MEM+(w-4)*2048+1024+l*16)=v1; }
    __syncthreads();

    if(FIN){ dl=now()+P.dB;
      if(s<NSEG-1){ if(have<s+1) have=__builtin_amdgcn_readfirstlane(waitprog(flg,32+c,s+1,dead,1));
        vo0=cat(LD128(vor,voo,16),LD128(vor,voo+1024,16)); vo1=cat(LD128(vor,voo+2048,16),LD128(vor,voo+3072,16)); voo+=NV*8*2048; } }
    f32x4 c0={0,0,0,0},c1={0,0,0,0}; u32x4 am[7];
    #pragma unroll
    for(int t=0;t<7;t++) am[t]=*(const u32x4*)(lds+L_MEM+t*1024+l*16);
    __builtin_amdgcn_sched_barrier(0);
    #pragma unroll
    for(int t=0;t<7;t++){ c0=smf64(am[t],W[0][t],c0,idx); c1=smf64(am[t],W[1][t],c1,idx); }
    u32x2 u={canon(pk(swadd(c0[0],c1[0]),swadd(c0[1],c1[1]))), canon(pk(swadd(c0[2],c1[2]),swadd(c0[3],c1[3])))};
    ST64(u,upr,((s*NV+c)*8+pr)*512+l*8,16);
    if(s==NSEG-1) break;
    if(!FIN) *(u32x2*)ux=u;
    __syncthreads();

    if(FIN){
      u32x2 u2=*(const u32x2*)ux;
      u32x4 e0,e1,p0,p1;
      until(dl-P.dX);
      u32 Lx=LD32(hot,base+(oL^X),16); e0=LD128(hot,base+(oEX^X),16); e1=LD128(hot,base+((oEX+1024)^X),16);
      until(dl);
      p0=LD128(hot,base+32768+(oSM^X),16); p1=LD128(hot,base+32768+((oSM+256)^X),16);
      if(anysent(max(Lx,max8(e0,e1)))){
        #pragma unroll 1
        do{ asm volatile("":::"memory"); Lx=LD32(hot,base+(oL^X),16); e0=LD128(hot,base+(oEX^X),16); e1=LD128(hot,base+((oEX+1024)^X),16); } while(anysent(max(Lx,max8(e0,e1))) && --dead>0); --dead; }
      f32x4 cx0=smf64(e0,vo0,f32x4{0,0,0,0},idx), cx1=smf64(e1,vo1,f32x4{0,0,0,0},idx);
      if(anysent(max8(p0,p1))){
        #pragma unroll 1
        do{ asm volatile("":::"memory"); p0=LD128(hot,base+32768+(oSM^X),16); p1=LD128(hot,base+32768+((oSM+256)^X),16); } while(anysent(max8(p0,p1)) && --dead>0); --dead; }

      f32x4 t=BC(f32x4,p0)+(BC(f32x4,p1)-BC(float,Lx));
      float m=vmax3(vmax3(t[0],t[1],t[2]),t[3],0.f); m=maxdpp<0x128>(m);
      float a0=__builtin_amdgcn_exp2f(t[0]-m),a1=__builtin_amdgcn_exp2f(t[1]-m),a2=__builtin_amdgcn_exp2f(t[2]-m),a3=__builtin_amdgcn_exp2f(t[3]-m),f=__builtin_amdgcn_exp2f(-m);
      float sm=(a0+a1)+(a2+a3); sm+=dpp<0x128>(sm);
      float rl=__builtin_amdgcn_rcpf(sm+f), fl=f*rl; u32 ff=pk(fl,fl);
      u32x8 bz={u.x,u.y,pk(cx0[0],cx0[1]),pk(cx0[2],cx0[3]),u2.x,u2.y,pk(cx1[0],cx1[1]),pk(cx1[2],cx1[3])};
      f32x4 o=smf64(u32x4{pk(a0*rl,a1*rl),ff&dm0,pk(a2*rl,a3*rl),ff&dm1},bz,f32x4{0,0,0,0},idx);
      redw[0]=swadd(o[0],o[2]); redw[8]=swadd(o[1],o[3]);
    }
    __syncthreads();
    if(w==7){
      f32x2 a=(*(const f32x2*)redr+*(const f32x2*)(redr+576))+(*(const f32x2*)(redr+2*576)+*(const f32x2*)(redr+3*576));
      ST32(canon(pk(a[0],a[1])),hot,base+65536+((c*256+l*4)^X1),16);
    }

    if(!FIN) dl=now()+(w==7?P.dC-150:P.dC);
  }

}

#define ST128(v,r,o,aux) __builtin_amdgcn_raw_buffer_store_b128(v,r,o,0,aux)
constexpr int XROW=912, XBUF=32*XROW;
struct XStage{ u32x4 v[4]; int la[4]; };
DEV void xs_init(XStage& X,int tid){
  #pragma unroll
  for(int i=0;i<4;i++){ int p=min(tid+512*i,1791); X.la[i]=(p/56)*XROW+(p%56)*16; } }
DEV void xs_load(XStage& X,__amdgpu_buffer_rsrc_t xrs,int s,int tid){
  #pragma unroll
  for(int i=0;i<4;i++) X.v[i]=LD128(xrs,s*28672+min(tid+512*i,1791)*16,0); }
DEV void xs_put(const XStage& X,char* buf,int tid){
  #pragma unroll
  for(int i=0;i<4;i++) if(i<3||tid<256) *(u32x4*)(buf+X.la[i])=X.v[i]; }
DEV u32x4 pk8(const f32x16& a,int o){ return u32x4{pk(a[o],a[o+1]),pk(a[o+2],a[o+3]),pk(a[o+4],a[o+5]),pk(a[o+6],a[o+7])}; }

template<int SX>
DEV void kq_worker(const ChainArgs& P,char* lds,int slot,int w,int l,int tid){
  char* ws=P.ws; const int h=slot>>1,j=slot&1,m=l&31,hh=l>>5;
  auto xrs=mk(P.x,S*D*2); auto kq=mk(ws+O_KQ,NSEG*H*14*2048); auto sxx=mk(ws+O_SXX,NSEG*H*J*4096); auto flg=mk(ws+O_FLG,16384);
  XStage X; xs_init(X,tid); xs_load(X,xrs,0,tid);
  u32x4 W[28];
  if(!SX){ const char* p=ws+O_A+wf(h,224*j+32*w+sw23(m),hh);
    #pragma unroll
    for(int t=0;t<28;t++) W[t]=*(const u32x4*)(p+1024*t); }
  xs_put(X,lds,tid); xs_load(X,xrs,1,tid);
  __syncthreads();
  char* kl=lds+3*XBUF;
  int b0=0,b1=XBUF,b2=2*XBUF;
  for(int s=0;s<=NSEG;s++){
    xs_put(X,lds+b1,tid); xs_load(X,xrs,min(s+2,NSEG-1),tid);
    __builtin_amdgcn_sched_barrier(0);
    f32x16 c;
    #pragma unroll
    for(int i=0;i<16;i++) c[i]=0.f;
    if(!SX){ if(s<NSEG){
      const char* xp=lds+b0+m*XROW+hh*16;
      u32x4 f[6];
      #pragma unroll
      for(int t=0;t<6;t++) f[t]=*(const u32x4*)(xp+32*t);
      #pragma unroll
      for(int t=0;t<28;t++){ __builtin_amdgcn_sched_barrier(0); c=mf32(W[t],f[t%6],c); if(t+6<28) f[t%6]=*(const u32x4*)(xp+32*(t+6)); }
      __builtin_amdgcn_sched_barrier(0);
      u32x4 k0=pk8(c,0),k1=pk8(c,8);
      vmwait();
      const u32 o=(((s*H+h)*2+(m>>4))*14+7*j+w)*1024+((m&15)+16*hh)*16;
      ST128(k0,kq,o,16); ST128(k1,kq,o+512,16);
      char* q=kl+(s&1)*14336+w*2048+l*16; *(u32x4*)q=k0; *(u32x4*)(q+1024)=k1; } }
    else if(s>0){
      const char* xp=lds+b2+m*XROW+448*j+hh*16; const char* q=kl+((s-1)&1)*14336+l*16;
      u32x4 f[14],e[14];
      #pragma unroll
      for(int t=0;t<14;t++){ e[t]=*(const u32x4*)(q+1024*t); f[t]=*(const u32x4*)(xp+32*t); }
      __builtin_amdgcn_sched_barrier(0);
      #pragma unroll
      for(int t=0;t<14;t++) c=mf32(e[t],f[t],c);
      vmwait();
      const u32 o=(((s-1)*H+h)*2+j)*4096+hh*512+m*16;
      #pragma unroll
      for(int q4=0;q4<4;q4++) ST128(BC(u32x4,(f32x4{c[4*q4],c[4*q4+1],c[4*q4+2],c[4*q4+3]})),sxx,o+q4*1024,16);
    }
    if(s<3||s==NSEG) vmwait();
    __syncthreads();
    if(tid==0) ST32((u32)((s<3||s==NSEG)?s+1:s),flg,slot*128,16);
    int t=b2; b2=b0; b0=b1; b1=t;
  }
}

DEV void vo_worker(const ChainArgs& P,char* lds,int c,int w,int l,int tid){
  char* ws=P.ws; const int r=l&15,g=l>>4;
  auto xrs=mk(P.x,S*D*2); auto vor=mk(ws+O_VO,NSEG*NV*8*2048); auto flg=mk(ws+O_FLG,16384);
  XStage X; xs_init(X,tid); xs_load(X,xrs,0,tid);
  u32x4 W[2][14];
  #pragma unroll
  for(int hh=0;hh<2;hh++){ const char* p=ws+O_BT+wf(2*w+hh,16*c+r,g);
    #pragma unroll
    for(int t=0;t<14;t++) W[hh][t]=*(const u32x4*)(p+2048*t); }
  xs_put(X,lds,tid); xs_load(X,xrs,1,tid);
  __syncthreads();
  for(int s=0;s<NSEG;s++){
    xs_put(X,lds+((s+1)&1)*XBUF,tid); xs_load(X,xrs,min(s+2,NSEG-1),tid);
    __builtin_amdgcn_sched_barrier(0);
    f32x4 a[2][2]={{{0,0,0,0},{0,0,0,0}},{{0,0,0,0},{0,0,0,0}}};
    const char* xp=lds+(s&1)*XBUF+r*XROW+g*16;
    u32x4 f[6];
    #pragma unroll
    for(int t=0;t<6;t++) f[t]=*(const u32x4*)(xp+(t&1)*16*XROW+64*(t>>1));
    #pragma unroll
    for(int t=0;t<28;t++){ __builtin_amdgcn_sched_barrier(0); a[t&1][0]=mf16(f[t%6],W[0][t>>1],a[t&1][0]); a[t&1][1]=mf16(f[t%6],W[1][t>>1],a[t&1][1]);
      if(t+6<28) f[t%6]=*(const u32x4*)(xp+(t&1)*16*XROW+64*((t+6)>>1)); }
    __builtin_amdgcn_sched_barrier(0);
    vmwait();
    #pragma unroll
    for(int hf=0;hf<2;hf++) ST128((u32x4{pk(a[hf][0][0],a[hf][0][1]),pk(a[hf][1][0],a[hf][1][1]),pk(a[hf][0][2],a[hf][0][3]),pk(a[hf][1][2],a[hf][1][3])}),vor,(((s*NV+c)*8+w)*2+hf)*1024+l*16,16);
    if(s<3||s==NSEG-1) vmwait();
    __syncthreads();
    if(tid==0) ST32((u32)((s<3||s==NSEG-1)?s+1:s),flg,(32+c)*128,16);
  }
}

DEV float sum16(float v){ v+=dpp<0xB1>(v); v+=dpp<0x4E>(v); v+=dpp<0x141>(v); v+=dpp<0x140>(v); return v; }
DEV float gelu(float a){ float u=a*(1.5957691216057308f+0.07135481282f*a*a); return a*__builtin_amdgcn_rcpf(1.f+__builtin_amdgcn_exp2f(-1.4426950408889634f*u)); }
DEV void mlp_worker(const ChainArgs& P,char* lds,int s,int w,int l,int tid){
  char* ws=P.ws; const int m=l&31,hh=l>>5;
  char* HL=lds+XBUF+256; float* GL=(float*)(lds+XBUF);
  { auto wr=mk(ws+O_W1,2*D*F*2);
    #pragma unroll 1
    for(int k0=tid;k0<3136;k0+=512){ const int id=s*3136+k0; const bool two=id>=100352; const int k=two?id-100352:id, ln=k&63, mm=ln&31, h2=ln>>5, fr=k>>6;
      const u16* p; int str;
      if(!two){ p=P.w1+(size_t)(16*(fr%28)+8*h2)*F+32*(fr/28)+sw23(mm); str=F; } else { p=P.w2+(size_t)(16*(fr%112)+8*h2)*D+32*(fr/112)+mm; str=D; }
      u32 e[8];
      #pragma unroll
      for(int j=0;j<8;j++) e[j]=p[j*str];
      ST128((u32x4{e[0]|(e[1]<<16),e[2]|(e[3]<<16),e[4]|(e[5]<<16),e[6]|(e[7]<<16)}),wr,id*16,16); }
    vmwait(); __syncthreads();
    if(tid==0) ST32(1u,mk(ws+O_FLG,32768),(128+s)*128,16); }
  { const int row=4*w+(l>>4), q=l&15; const u16* xr=P.x+(size_t)(s*32+row)*D;
    float v[4][8]; float sm=0,gd=0;
    #pragma unroll
    for(int i=0;i<4;i++){ const bool ok=(i<3||q<8); const int ch=ok?q+16*i:0; u32x4 a=*(const u32x4*)(xr+ch*8), b=*(const u32x4*)(P.gw+ch*8);
      #pragma unroll
      for(int k=0;k<4;k++){ u32 av=ok?a[k]:0u; u32 bv=b[k]; v[i][2*k]=lo16(av); v[i][2*k+1]=hi16(av); sm+=v[i][2*k]+v[i][2*k+1]; gd+=v[i][2*k]*lo16(bv)+v[i][2*k+1]*hi16(bv); } }
    sm=sum16(sm); gd=sum16(gd); const float mean=sm*(1.f/D); float vr=0;
    #pragma unroll
    for(int i=0;i<4;i++){ const bool ok=(i<3||q<8);
      #pragma unroll
      for(int k=0;k<8;k++){ v[i][k]-=mean; vr+=ok?v[i][k]*v[i][k]:0.f; } }
    vr=sum16(vr); const float rs=1.f/sqrtf(vr*(1.f/D)+1e-5f);
    #pragma unroll
    for(int i=0;i<4;i++) if(i<3||q<8) *(u32x4*)(lds+row*XROW+(q+16*i)*16)=u32x4{pk(v[i][0]*rs,v[i][1]*rs),pk(v[i][2]*rs,v[i][3]*rs),pk(v[i][4]*rs,v[i][5]*rs),pk(v[i][6]*rs,v[i][7]*rs)};
    const float gt=1.f/(1.f+__expf(-gd));
    if(q==0){ GL[row]=1.f-gt; ST32(BC(u32,gt),mk(ws+O_G,S*4),(s*32+row)*4,16); }
  }
  { int dead=SPIN_MAX; waitprog(mk(ws+O_FLG,32768),128+l,1,dead,8); __builtin_amdgcn_fence(__ATOMIC_ACQUIRE,"agent"); }
  __syncthreads();
  { const char* yp=lds+m*XROW+hh*16;
    #pragma unroll 1
    for(int ft=w;ft<56;ft+=8){
      const char* p=ws+O_W1+(size_t)ft*28672+l*16; u32x4 Wt[28];
      #pragma unroll
      for(int t=0;t<28;t++) Wt[t]=*(const u32x4*)(p+1024*t);
      u32x4 ba=*(const u32x4*)(P.b1+32*ft+8*hh), bb=*(const u32x4*)(P.b1+32*ft+16+8*hh);
      f32x16 c;
      #pragma unroll
      for(int k=0;k<4;k++){ u32 x0=ba[k],x1=bb[k]; c[2*k]=lo16(x0); c[2*k+1]=hi16(x0); c[8+2*k]=lo16(x1); c[9+2*k]=hi16(x1); }
      #pragma unroll
      for(int t=0;t<28;t++) c=mf32(Wt[t],*(const u32x4*)(yp+32*t),c);
      #pragma unroll
      for(int k=0;k<16;k++) c[k]=gelu(c[k]);
      *(u32x4*)(HL+ft*2048+l*16)=pk8(c,0); *(u32x4*)(HL+ft*2048+1024+l*16)=pk8(c,8);
    } }
  __syncthreads();
  if(w<7){
    f32x16 c[2];
    #pragma unroll
    for(int i=0;i<2;i++){
      #pragma unroll
      for(int q=0;q<4;q++){ u32x2 b=*(const u32x2*)(P.b2+64*w+32*i+8*q+4*hh); u32 x0=b.x,x1=b.y; c[i][4*q]=lo16(x0); c[i][4*q+1]=hi16(x0); c[i][4*q+2]=lo16(x1); c[i][4*q+3]=hi16(x1); } }
    #pragma unroll 1
    for(int kb=0;kb<8;kb++){
      const char* p=ws+O_W2+(size_t)(2*w*112+kb*14)*1024+l*16; u32x4 Wt[2][14];
      #pragma unroll
      for(int t=0;t<14;t++){ Wt[0][t]=*(const u32x4*)(p+1024*t); Wt[1][t]=*(const u32x4*)(p+112*1024+1024*t); }
      #pragma unroll
      for(int t=0;t<14;t++){ u32x4 hb=*(const u32x4*)(HL+(kb*14+t)*1024+l*16); c[0]=mf32(Wt[0][t],hb,c[0]); c[1]=mf32(Wt[1][t],hb,c[1]); }
    }
    const float og=GL[m]; auto bbr=mk(ws+O_BB,S*D*4);
    #pragma unroll
    for(int i=0;i<2;i++){
      #pragma unroll
      for(int q=0;q<4;q++) ST128(BC(u32x4,(f32x4{c[i][4*q]*og,c[i][4*q+1]*og,c[i][4*q+2]*og,c[i][4*q+3]*og})),bbr,((s*112+16*w+8*i+2*q+hh)*32+m)*16,16); }
  }
  vmwait(); __syncthreads();
  if(tid==0) ST32(1u,mk(ws+O_FLG,16384),(64+s)*128,16);
}

DEV float max16x(float v){ auto r=__builtin_amdgcn_permlane16_swap(BC(u32,v),BC(u32,v),false,false); u32 a=r[0],b=r[1]; return vmax3(BC(float,a),BC(float,b),BC(float,b)); }
DEV float add16x(float v){ auto r=__builtin_amdgcn_permlane16_swap(BC(u32,v),BC(u32,v),false,false); u32 a=r[0],b=r[1]; return BC(float,a)+BC(float,b); }
DEV float max32x(float v){ auto r=__builtin_amdgcn_permlane32_swap(BC(u32,v),BC(u32,v),false,false); u32 a=r[0],b=r[1]; return vmax3(BC(float,a),BC(float,b),BC(float,b)); }
DEV void out_worker(const ChainArgs& P,char* lds,int sg,int cg,int w,int l,int tid){
  char* ws=P.ws; const int r=l&15,g=l>>4; int dead=SPIN_MAX;
  auto vor=mk(ws+O_VO,NSEG*NV*8*2048); auto flg=mk(ws+O_FLG,16384); auto sxx=mk(ws+O_SXX,NSEG*H*J*4096);
  auto sxm=mk(ws+O_SXM,NSEG*H*J*1024); auto upr=mk(ws+O_UP,NSEG*NV*8*512); auto bbr=mk(ws+O_BB,S*D*4); auto gr=mk(ws+O_G,S*4); auto outr=mk(P.out,S*D*2);
  #pragma unroll 1
  for(int s=sg;s<NSEG;s+=8){
    waitprog(flg,l<4?4*w+l:(l<8?32+4*cg+(l-4):64+s),l<4?s+2:(l<8?s+1:1),dead,16);
    f32x4 sv[2][2][2]; u32x4 vv[4][2];
    #pragma unroll
    for(int hh=0;hh<2;hh++){ const u32 o=((s*H+2*w+hh)*2)*4096+g*512+r*16;
      sv[hh][0][0]=BC(f32x4,LD128(sxx,o,16))+BC(f32x4,LD128(sxx,o+4096,16));
      sv[hh][1][0]=BC(f32x4,LD128(sxx,o+256,16))+BC(f32x4,LD128(sxx,o+4096+256,16));
      sv[hh][1][1]=BC(f32x4,LD128(sxx,o+2048+256,16))+BC(f32x4,LD128(sxx,o+4096+2048+256,16)); }
    #pragma unroll
    for(int ct=0;ct<4;ct++){ const u32 o=((s*NV+4*cg+ct)*8+w)*2048+l*16; vv[ct][0]=LD128(vor,o,16); vv[ct][1]=LD128(vor,o+1024,16); }
    const int ort=w>>2, oct=w&3;
    const f32x4 bb=BC(f32x4,LD128(bbr,((s*112+16*cg+4*oct+g)*32+16*ort+r)*16,16));
    const float gt=BC(float,__builtin_amdgcn_raw_buffer_load_b32(gr,(s*32+16*ort+r)*4,0,16));
    f32x4 tmp[2][2][4]; float mx[2],lx[2];
    #pragma unroll
    for(int hh=0;hh<2;hh++){
      #pragma unroll
      for(int rt=0;rt<2;rt++){
        f32x4 a=sv[hh][rt][0], b=sv[hh][rt][rt];
        #pragma unroll
        for(int i=0;i<4;i++) b[i]=(4*g+i<=r)?b[i]:-3.0e38f;
        float mm=vmax3(vmax3(b[0],b[1],b[2]),b[3],b[3]); if(rt) mm=vmax3(vmax3(a[0],a[1],mm),a[2],a[3]);
        mm=max32x(max16x(mm)); const float mc=mm*CSC; float sum=0;
        #pragma unroll
        for(int i=0;i<4;i++){ b[i]=(4*g+i<=r)?__builtin_amdgcn_exp2f(b[i]*CSC-mc):0.f; sum+=b[i]; if(rt){ a[i]=__builtin_amdgcn_exp2f(a[i]*CSC-mc); sum+=a[i]; } }
        sum=add16x(sum); sum=swadd(sum,sum);
        if(hh==0||g>=2){ mx[rt]=mc; lx[rt]=sum; }
        const u32x4 e= rt? u32x4{pk(a[0],a[1]),pk(a[2],a[3]),pk(b[0],b[1]),pk(b[2],b[3])} : u32x4{pk(b[0],b[1]),pk(b[2],b[3]),0u,0u};
        #pragma unroll
        for(int ct=0;ct<4;ct++) tmp[hh][rt][ct]=mf16(u32x4{vv[ct][0][hh],vv[ct][0][2+hh],vv[ct][1][hh],vv[ct][1][2+hh]},e,f32x4{0,0,0,0});
      } }
    const u32 om=((s*H+2*w+(g>>1))*2)*1024+(g&1)*256+r*16, ou=((s*NV+4*cg)*8+w)*512+l*8;
    { u32 v;
      #pragma unroll 1
      while(true){ asm volatile("":::"memory"); v=__builtin_amdgcn_raw_buffer_load_b32(sxm,((s*H+2*w)*2)*1024,0,16); if(__builtin_amdgcn_readfirstlane(v)!=SENT || --dead<=0) break; nap(4); } }
    u32x4 q[2][2]; u32x2 uu[4];
    #pragma unroll 1
    while(true){ asm volatile("":::"memory"); u32 mxs=0;
      #pragma unroll
      for(int rt=0;rt<2;rt++){ q[rt][0]=LD128(sxm,om+rt*512,16); q[rt][1]=LD128(sxm,om+1024+rt*512,16); }
      #pragma unroll
      for(int ct=0;ct<4;ct++) uu[ct]=LD64(upr,ou+ct*4096,16);
      #pragma unroll
      for(int rt=0;rt<2;rt++){ mxs=umax3(umax3(mxs,q[rt][0].x,q[rt][0].y),q[rt][0].z,q[rt][0].w); mxs=umax3(umax3(mxs,q[rt][1].x,q[rt][1].y),q[rt][1].z,q[rt][1].w); }
      #pragma unroll
      for(int ct=0;ct<4;ct++) mxs=umax3(mxs,uu[ct].x,uu[ct].y);
      if(!anysent(mxs) || --dead<=0) break; nap(2); }
    #pragma unroll
    for(int rt=0;rt<2;rt++){
      f32x4 t=(BC(f32x4,q[rt][0])+BC(f32x4,q[rt][1]))*CSC;
      float mm=max16x(vmax3(vmax3(t[0],t[1],t[2]),t[3],mx[rt]));
      float a0=__builtin_amdgcn_exp2f(t[0]-mm),a1=__builtin_amdgcn_exp2f(t[1]-mm),a2=__builtin_amdgcn_exp2f(t[2]-mm),a3=__builtin_amdgcn_exp2f(t[3]-mm),f=__builtin_amdgcn_exp2f(mx[rt]-mm);
      const float rl=__builtin_amdgcn_rcpf(add16x((a0+a1)+(a2+a3))+f*lx[rt]), sc=f*rl;
      auto sp=__builtin_amdgcn_permlane32_swap(BC(u32,sc),BC(u32,sc),false,false); u32 s0=sp[0],s1=sp[1]; const float sA=BC(float,s0),sB=BC(float,s1);
      const u32x2 pm={pk(a0*rl,a1*rl),pk(a2*rl,a3*rl)};
      #pragma unroll
      for(int ct=0;ct<4;ct++) *(f32x4*)(lds+((w*8+rt*4+ct)*64+l)*16)=mf16k(uu[ct],pm,tmp[0][rt][ct]*sA+tmp[1][rt][ct]*sB);
    }
    __syncthreads();
    f32x4 o=*(const f32x4*)(lds+(w*64+l)*16);
    #pragma unroll
    for(int k=1;k<8;k++) o+=*(const f32x4*)(lds+((k*8+w)*64+l)*16);
    o=o*gt+bb;
    ST64((u32x2{pk(o[0],o[1]),pk(o[2],o[3])}),outr,((s*32+16*ort+r)*D+64*cg+16*oct+4*g)*2,0);
    __syncthreads();
  }
}

#define XS 3
#define MAP 4
constexpr int XV=XS^2, XK=XS^1, XO=XS^3;
constexpr int LDS2=XBUF+256+114688;
__global__ __launch_bounds__(512,2) void k_main(ChainArgs P){
  extern __shared__ __attribute__((aligned(16))) char lds[];
  const int rot=*(const int*)(P.ws+O_XOR)&7;
  const int b=blockIdx.x, xcd=(b+rot)&7, slot=b>>3, cls=((XS&4)-rot)&7;
  const int tid=threadIdx.x, l=tid&63, w=__builtin_amdgcn_readfirstlane(tid>>6);

  const int q=xcd&3, role=((xcd^XS)&4)?4:slot>>3, sl=q*8+(slot&7), c=(slot&7)<7?q*7+(slot&7):-1;
  if(role==0){
    if(w==0) score_loop<0>(P,lds,sl,w,l,cls);
    else if(w<3) score_loop<1>(P,lds,sl,w,l,cls);
    else if(w<7) score_loop<2>(P,lds,sl,w,l,cls);
    else if((sl&1)==0) score_loop<3>(P,lds,sl,w,l,cls);
    else score_loop<4>(P,lds,sl,w,l,cls);
  }
  else if(role==1){ if(c>=0){ if(w<4) value_loop<1>(P,lds,c,w,l,cls); else value_loop<0>(P,lds,c,w,l,cls); } }
  else if(role==2){ if(w<7) kq_worker<0>(P,lds,sl,w,l,tid); else kq_worker<1>(P,lds,sl,w,l,tid); }
  else if(role==3){ if(c>=0) vo_worker(P,lds,c,w,l,tid); }
  else if(((xcd^XS)&6)==4) mlp_worker(P,lds,slot*2+(xcd&1),w,l,tid);
  else if(slot<28) out_worker(P,lds,(slot/7)*2+(xcd&1),slot%7,w,l,tid);

}

struct PreArgs{ const u16* mem0; const u16* Wq; const u16* Wk; const u16* Wv; const u16* Wo; const u16* w1; const u16* w2; char* ws; };
template<int WH>
DEV void ab_gemm(const PreArgs& P,char* lds,int h,int mb,int w,int l,int tid){
  const int m=l&31,hh=l>>5; u32x4 st[4][8];
  const u16* Np=(WH?P.Wv:P.Wk)+(size_t)(tid>>3)*4096+h*256+(tid&7)*8;
  const u16* Mp= WH? P.Wo+(size_t)(h*256+(tid>>3))*D+64*mb+(tid&7)*8 : P.Wq+(size_t)(64*mb+(tid>>3))*4096+h*256+(tid&7)*8;
  #pragma unroll
  for(int q=0;q<4;q++){
    #pragma unroll
    for(int i=0;i<7;i++) st[q][i]=*(const u32x4*)(Np+(size_t)i*64*4096+64*q);
    st[q][7]=*(const u32x4*)(Mp+(WH?64*D:64)*q); }
  const int la=(tid>>3)*128+(((tid&7)^((tid>>4)&7))*16), lam=WH?57344+tid*16:57344+la;
  f32x16 c[2][2];
  #pragma unroll
  for(int i=0;i<16;i++){ c[0][0][i]=0;c[0][1][i]=0;c[1][0][i]=0;c[1][1][i]=0; }
  const int ra=sw23(m);
  #pragma unroll
  for(int hk=0;hk<2;hk++){
    if(hk) __syncthreads();
    #pragma unroll
    for(int q=0;q<2;q++){
      #pragma unroll
      for(int i=0;i<7;i++) *(u32x4*)(lds+q*65536+i*8192+la)=st[2*hk+q][i];
      *(u32x4*)(lds+q*65536+lam)=st[2*hk+q][7]; }
    __syncthreads();
    if(w<7){
      #pragma unroll
      for(int q=0;q<2;q++){
        #pragma unroll
        for(int t=0;t<4;t++){ u32x4 a[2],b[2];
          #pragma unroll
          for(int i=0;i<2;i++){
            a[i]=*(const u32x4*)(lds+q*65536+(64*w+32*i+ra)*128+(((2*t+hh)^((ra>>1)&7))*16));
            if(WH){ const char* p=lds+q*65536+57344+(16*t+8*hh+((l&15)>>2))*128+(32*i+16*((l>>4)&1)+4*(l&3))*2; u32x2 x0=trread(p),x1=trread(p+512); b[i]=u32x4{x0.x,x0.y,x1.x,x1.y}; }
            else b[i]=*(const u32x4*)(lds+q*65536+57344+(32*i+m)*128+(((2*t+hh)^((m>>1)&7))*16)); }
          c[0][0]=mf32(a[0],b[0],c[0][0]); c[0][1]=mf32(a[0],b[1],c[0][1]); c[1][0]=mf32(a[1],b[0],c[1][0]); c[1][1]=mf32(a[1],b[1],c[1][1]);
        } } } }
  if(w<7){ char* o=P.ws+(WH?O_BT:O_A);
    #pragma unroll
    for(int ti=0;ti<2;ti++){
      #pragma unroll
      for(int tj=0;tj<2;tj++){ char* p=o+wf(h,64*mb+32*tj+m,8*w+4*ti+hh); *(u32x4*)p=pk8(c[ti][tj],0); *(u32x4*)(p+1024)=pk8(c[ti][tj],8); } } }
}
__global__ __launch_bounds__(512,2) void k_pre(PreArgs P){
  extern __shared__ __attribute__((aligned(16))) char lds[];
  const int b=blockIdx.x, xcd=b&7, slot=b>>3;
  const int tid=threadIdx.x, l=tid&63, w=__builtin_amdgcn_readfirstlane(tid>>6);
  char* ws=P.ws; const u32x4 sent={SENT,SENT,SENT,SENT}; auto hot=mk(ws+O_HOT,66*65536);

  if(slot<28){ const int gm=xcd*4+slot/7; if(gm&1) ab_gemm<1>(P,lds,gm>>1,slot%7,w,l,tid); else ab_gemm<0>(P,lds,gm>>1,slot%7,w,l,tid); }
  else {
    const int mi=(slot-28)*8+xcd;
    {
      u32* p=(u32*)(ws+O_HOT+(size_t)w*524288+32768+(slot-28)*8192+xcd*1024); u32 best=0xFFFFFu,v;
      asm volatile("global_store_dword %0, %1, off sc1\n s_waitcnt vmcnt(0)"::"v"(p+(l&7)*32),"v"(SENT):"memory");
      __syncthreads();
      const u32 tb=now();
      for(int i=0;i<CALN;i++){ until(tb+(i*8+w)*CALT); u32 t0=now(); asm volatile("global_load_dword %0, %1, off sc1\n s_waitcnt vmcnt(0)":"=v"(v):"v"(p+i*32):"memory"); best=min(best,now()-t0); }
      if(l==0) ((u32*)(ws+O_XOR))[16+xcd*32+(slot-28)*8+w]=best;
    }
    if(b==224 && tid==0){ u32 v; asm volatile("s_getreg_b32 %0, hwreg(HW_REG_XCC_ID)":"=s"(v)); *(u32*)(ws+O_XOR)=v&7; }
    if(mi==1){ for(int k=tid;k<1792;k+=512){ int c=k>>6, hi=(k>>5)&1, i=(k>>2)&7, q=k&3; u32 v=canon(*(const u32*)(P.mem0+i*D+16*c+4*q+2*hi)); ST32(v,hot,64*65536+k*4,16); }
      if(tid<192) ST32(0u,mk(ws+O_FLG,32768),tid*128,16); }
  }

  for(u32 i=slot*512+tid;i<64*512;i+=32*512) ST128(sent,hot,(i>>5)*4096+xcd*512+(i&31)*16,16);
  for(u32 i=slot*512+tid;i<(O_SXX-O_UP)/128;i+=32*512) ST128(sent,mk(ws+O_UP,(u32)(O_SXX-O_UP)),(i>>5)*4096+xcd*512+(i&31)*16,16);

}
extern "C" void kernel_launch(void* const* d_in,const int* in_sizes,int n_in,void* d_out,int out_size,void* d_ws,size_t ws_size,hipStream_t stream){
  char* ws=(char*)(((size_t)d_ws+2097151)&~(size_t)2097151);
  (void)hipFuncSetAttribute((const void*)k_pre,hipFuncAttributeMaxDynamicSharedMemorySize,131072);
  (void)hipFuncSetAttribute((const void*)k_main,hipFuncAttributeMaxDynamicSharedMemorySize,LDS2);
  PreArgs Q{(const u16*)d_in[1],(const u16*)d_in[2],(const u16*)d_in[3],(const u16*)d_in[4],(const u16*)d_in[5],(const u16*)d_in[8],(const u16*)d_in[10],ws};
  k_pre<<<256,512,131072,stream>>>(Q);
  ChainArgs P{(const u16*)d_in[0],ws,(const u16*)d_in[9],(const u16*)d_in[11],(const u16*)d_in[12],(const u16*)d_in[8],(const u16*)d_in[10],(u16*)d_out,2100,1250,550,2400,200,0,0,0};
  k_main<<<256,512,LDS2,stream>>>(P);
}
```

```cpp
#include <hip/hip_runtime.h>
#include <hip/hip_bf16.h>
typedef unsigned u32; typedef unsigned long long u64; typedef unsigned short u16;
typedef u32 u32x2 __attribute__((ext_vector_type(2)));
typedef u32 u32x4 __attribute__((ext_vector_type(4)));
typedef u32 u32x8 __attribute__((ext_vector_type(8)));
typedef float f32x2 __attribute__((ext_vector_type(2)));
typedef float f32x4 __attribute__((ext_vector_type(4)));
typedef __bf16 bf16x2 __attribute__((ext_vector_type(2)));
typedef __bf16 bf16x8 __attribute__((ext_vector_type(8)));
typedef __bf16 bf16x16 __attribute__((ext_vector_type(16)));
typedef float f32x16 __attribute__((ext_vector_type(16)));
typedef short s16x4 __attribute__((ext_vector_type(4)));
typedef short s16x8 __attribute__((ext_vector_type(8)));
#define DEV __device__ __forceinline__
template<class T,class U> __device__ __forceinline__ T bcf(U x){ return __builtin_bit_cast(T,x); }
#define BC(T,x) bcf<T>(x)

constexpr int D=448, H=16, DH=256, S=2048, NSEG=64, F=1792, J=2, NV=28;
constexpr float CSC = 1.4426950408889634f/16.0f;
constexpr size_t O_HOT=0;
constexpr size_t O_XOR=O_HOT+(size_t)66*65536;
constexpr size_t O_FLG=O_XOR+32768;
constexpr size_t O_A  =O_XOR+65536;
constexpr size_t O_BT =O_A+(size_t)H*D*D*2;
constexpr size_t O_KQ =O_BT+(size_t)H*D*D*2;
constexpr size_t O_VO =O_KQ+(size_t)NSEG*H*14*2048;
constexpr size_t O_UP =O_VO+(size_t)NSEG*NV*8*2048;
constexpr size_t O_SXM=O_UP+(size_t)NSEG*NV*8*512;
constexpr size_t O_SXX=O_SXM+(size_t)NSEG*H*J*1024;
constexpr size_t O_BB =O_SXX+(size_t)NSEG*H*J*4096;
constexpr size_t O_G  =O_BB+(size_t)S*D*4;
constexpr size_t O_W1 =O_G+(size_t)S*4;
constexpr size_t O_W2 =O_W1+(size_t)D*F*2;
constexpr size_t O_END=O_W2+(size_t)D*F*2;

DEV float bf2f(u16 v){ return BC(float,(u32)v<<16); }
DEV u16 f2bf(float f){ __bf16 b=(__bf16)f; return BC(u16,b); }
DEV u32 pk(float a,float b){ bf16x2 v={(__bf16)a,(__bf16)b}; return BC(u32,v); }
DEV __amdgpu_buffer_rsrc_t mk(const void* p, u32 bytes){ return __builtin_amdgcn_make_buffer_rsrc((void*)p,0,bytes,0x00020000); }
DEV u32 now(){ u64 t; asm volatile("s_memtime %0\n s_waitcnt lgkmcnt(0)":"=s"(t)::"memory"); return (u32)t; }
DEV f32x4 smf64(u32x4 a,u32x8 b,f32x4 c,int idx){ return __builtin_amdgcn_smfmac_f32_16x16x64_bf16(BC(bf16x8,a),BC(bf16x16,b),c,idx,0,0); }
DEV f32x4 smf32(u32x2 a,u32x4 b,f32x4 c,int idx){ return __builtin_amdgcn_smfmac_f32_16x16x32_bf16(BC(s16x4,a),BC(s16x8,b),c,idx,0,0); }
DEV float swadd(float a,float b){ auto r=__builtin_amdgcn_permlane32_swap(BC(u32,a),BC(u32,b),false,false); return BC(float,r[0])+BC(float,r[1]); }
template<int C> DEV float dpp(float x){ return BC(float,__builtin_amdgcn_update_dpp(0,BC(int,x),C,0xF,0xF,true)); }
DEV float vmax3(float a,float b,float c){ float r; asm("v_max3_f32 %0,%1,%2,%3":"=v"(r):"v"(a),"v"(b),"v"(c)); return r; }
template<int C> DEV float maxdpp(float x){ float y=dpp<C>(x); return vmax3(x,y,y); }
DEV u32x8 cat(u32x4 a,u32x4 b){ return __builtin_shufflevector(a,b,0,1,2,3,4,5,6,7); }
DEV u32 canon(u32 x){ return x>0xFFFFFFFEu?0xFFFFFFFEu:x; }
DEV u32x2 trread(const char* p){ return BC(u32x2,__builtin_amdgcn_ds_read_tr16_b64_v4i16((__attribute__((address_space(3))) s16x4*)p)); }
constexpr int SPIN_MAX=400000;
#define SENT 0xFFFFFFFFu
DEV u32 umax3(u32 a,u32 b,u32 c){ return max(max(a,b),c); }
DEV bool anysent(u32 m){ return __builtin_amdgcn_ballot_w64(m==SENT)!=0; }
DEV void nap(int n){ for(int i=0;i<n;i++) __builtin_amdgcn_s_sleep(1); }
DEV void until(u32 t){ int d; while((d=(int)(t-now()))>0){ if(d>160) __builtin_amdgcn_s_sleep(1); } }
DEV u32 max8(u32x4 a,u32x4 b){ return max(umax3(umax3(a.x,a.y,a.z),a.w,b.x),umax3(b.y,b.z,b.w)); }
DEV u32x4 poll16(__amdgpu_buffer_rsrc_t r,u32 off,int& budget){
  u32x4 v;
  #pragma unroll 1
  do { asm volatile("":::"memory"); v=__builtin_amdgcn_raw_buffer_load_b128(r,off,0,16);
  } while(anysent(max(umax3(v.x,v.y,v.z),v.w)) && --budget>0);
  return v;
}
#define CALT 320
#define CALN 4
#define OAUX 16
#define KAUX 16
DEV u32 loadX(const char* ws,int cls){ const int* xr=(const int*)(ws+O_XOR)+16; const int l=threadIdx.x&63; int t0=0;
  for(int q=0;q<4;q++) t0+=xr[((cls+4*(l>>5)+q)&7)*32+(l&31)];
  int t=(((l>>3)^(l>>5))&1)?-t0:t0;
  t+=__shfl_xor(t,8); t+=__shfl_xor(t,16); t+=__shfl_xor(t,32);
  int p=t+__shfl_xor(t,1); p+=__shfl_xor(p,2);
  const u32 m=(u32)__builtin_amdgcn_ballot_w64((t>960||t<-960)?t>0:p>0);
  u32 X=0; for(int i=0;i<8;i++)X|=((m>>i)&1u)<<(2*i); return X; }
#define XM(s) (((XX>>(((s)>>3)*2))&3u)<<13)

DEV f32x16 mf32(u32x4 a,u32x4 b,f32x16 c){ return __builtin_amdgcn_mfma_f32_32x32x16_bf16(BC(bf16x8,a),BC(bf16x8,b),c,0,0,0); }
DEV f32x4 mf16(u32x4 a,u32x4 b,f32x4 c){ return __builtin_amdgcn_mfma_f32_16x16x32_bf16(BC(bf16x8,a),BC(bf16x8,b),c,0,0,0); }
DEV f32x4 mf16k(u32x2 a,u32x2 b,f32x4 c){ return __builtin_amdgcn_mfma_f32_16x16x16bf16_1k(BC(s16x4,a),BC(s16x4,b),c,0,0,0); }
DEV int sw23(int m){ return (m&19)|((m&4)<<1)|((m&8)>>1); }
DEV u32 wf(int h,int row,int kc){ return (u32)(((h*14+(row>>5))*56+kc)*512+(row&31)*16); }
DEV void vmwait(){ asm volatile("":::"memory"); __builtin_amdgcn_s_waitcnt(0x0F70); asm volatile("":::"memory"); }
DEV float lo16(u32 v){ return BC(float,v<<16); }
DEV float hi16(u32 v){ return BC(float,v&0xFFFF0000u); }
DEV int waitprog(__amdgpu_buffer_rsrc_t fl,int idx,int need,int& budget,int slp){
  int v;
  #pragma unroll 1
  while(true){ asm volatile("":::"memory"); v=(int)__builtin_amdgcn_raw_buffer_load_b32(fl,idx*128,0,16);
    if(__builtin_amdgcn_ballot_w64(v<need)==0 || --budget<=0) break; nap(slp); }
  return v;
}

struct ChainArgs{ const u16* x; char* ws; const u16* b1; const u16* b2; const u16* gw; const u16* w1; const u16* w2; u16* out; int dA; int dB; int dC; int dD; int dE; int dX; int dG; int ts; };
constexpr int L_MEM=0, L_TT=7168, L_SX=L_TT+2*2112+64, L_L=L_SX+1024, L_RED=7168, L_U=L_RED+4096;

#define LD128(r,o,aux) __builtin_amdgcn_raw_buffer_load_b128(r,o,0,aux)
#define LD32(r,o,aux) __builtin_amdgcn_raw_buffer_load_b32(r,o,0,aux)
#define LD64(r,o,aux) __builtin_amdgcn_raw_buffer_load_b64(r,o,0,aux)
#define ST64(v,r,o,aux) __builtin_amdgcn_raw_buffer_store_b64(v,r,o,0,aux)
#define ST32(v,r,o,aux) __builtin_amdgcn_raw_buffer_store_b32(v,r,o,0,aux)

template<int ROLE>
DEV void score_loop(const ChainArgs& P,char* lds,int slot,int w,int l,int cls){
  const int r=l&15,g=l>>4; char* ws=P.ws;
  const int h=slot>>1, j=slot&1, n0=224*j;
  auto hot=mk(ws+O_HOT,66*65536);
  const u32 XX=loadX(ws,cls);
  const int idx=(r<8)?0x4444:0xEEEE;
  int dead=SPIN_MAX;
  u32x8 W[2][7];
  auto kq=mk(ws+O_KQ,NSEG*H*14*2048); auto flg=mk(ws+O_FLG,16384); int have=0;
  if(ROLE<3){
    #pragma unroll
    for(int tt=0;tt<2;tt++){ const char* p=ws+O_A+wf(h,n0+32*w+16*tt+r,g);
      #pragma unroll
      for(int t=0;t<7;t++) W[tt][t]=cat(*(const u32x4*)(p+4096*t),*(const u32x4*)(p+4096*t+2048)); }
  } else if(ROLE==3){
    { int v=waitprog(flg,2*h+(l&1),1,dead,1); have=min(__builtin_amdgcn_readlane(v,0),__builtin_amdgcn_readlane(v,1)); }
    #pragma unroll
    for(int k=0;k<14;k++){ u32 o=(u32)h*14*2048+k*2048+l*16; W[k/7][k%7]=cat(LD128(kq,o,16),LD128(kq,o+1024,16)); }
    __builtin_amdgcn_s_setprio(3);
  }
  if(ROLE==0){ *(u32x2*)(lds+L_TT+(l>>5)*2112+(224+(l&31))*8)=u32x2{0,0}; }
  if(ROLE==0) __builtin_amdgcn_s_setprio(2);
  u32x4 xb[7];
  auto xrs=mk(P.x,S*D*2);
  const u32 xo=(u32)((16*(w-1)+r)*D+n0+8*g)*2;
  if(ROLE==1){
    #pragma unroll
    for(int k=0;k<7;k++) xb[k]=LD128(xrs,xo+64*k,0);
  }
  const int rr=r>>2, hp=(l>>1)&1, q1=l&1;
  const char* trp=lds+L_TT+q1*2112+(32*(g>>1)+16*(g&1)+(ROLE==0?4*hp+rr:2*hp+(rr&1)+4*(rr>>1)))*8;
  auto sxm=mk(ws+O_SXM,NSEG*H*J*1024);
  __builtin_amdgcn_s_waitcnt(0x0F70);

  u32 dl=now();
  for(int s=0;s<NSEG;s++){
    const u32 X=XM(s); const u32 base=s*65536u;
    if(ROLE<3){ until(dl); u32x4 v=poll16(hot,(s?base:64*65536u)+((w*1024+l*16)^(s?X:0u)),dead); if(s) {  }  *(u32x4*)(lds+L_MEM+w*1024+l*16)=v; }
    __syncthreads();
    if(ROLE==4){  } else {  }
    float v0,v1,v2,v3,mc; u32x2 mb[14]; u32x4 am[7];
    if(ROLE<4){
      f32x4 c0={0,0,0,0},c1={0,0,0,0};
      #pragma unroll
      for(int t=0;t<7;t++) am[t]=*(const u32x4*)(lds+L_MEM+t*1024+l*16);
      __builtin_amdgcn_sched_barrier(0);
      #pragma unroll
      for(int t=0;t<7;t++){ c0=smf64(am[t],W[0][t],c0,idx); c1=smf64(am[t],W[1][t],c1,idx); }
      if(ROLE==0){
        #pragma unroll
        for(int k=0;k<7;k++){ const char* p=lds+L_MEM+(14*j+2*k+(g>>1))*256+(l&7)*16+(g&1)*8; mb[2*k]=*(const u32x2*)p; mb[2*k+1]=*(const u32x2*)(p+128); }
      }
      v0=swadd(c0[0],c1[0]);v1=swadd(c0[1],c1[1]);v2=swadd(c0[2],c1[2]);v3=swadd(c0[3],c1[3]);
      asm volatile("":"+v"(v0),"+v"(v3));
      if(ROLE<3) *(u32x2*)(lds+L_TT+(g&1)*2112+(32*w+16*(l>>5)+r)*8)=u32x2{pk(v0,v1),pk(v2,v3)};
      if(ROLE==3){
        float* sx=(float*)(lds+L_SX)+(4*(g&1))*32+16*(l>>5)+r;
        sx[0]=v0; sx[32]=v1; sx[64]=v2; sx[96]=v3;
        __builtin_amdgcn_wave_barrier();
        f32x4 z=*(const f32x4*)(lds+L_SX+l*16);
        float m=vmax3(vmax3(z[0],z[1],z[2]),z[3],z[3]);
        m=maxdpp<0xB1>(m); m=maxdpp<0x4E>(m); m=maxdpp<0x141>(m);
        mc=m*CSC;
        v0=__builtin_amdgcn_exp2f(z[0]*CSC-mc);v1=__builtin_amdgcn_exp2f(z[1]*CSC-mc);v2=__builtin_amdgcn_exp2f(z[2]*CSC-mc);v3=__builtin_amdgcn_exp2f(z[3]*CSC-mc);
        asm volatile("":"+v"(v0),"+v"(v3));
      }
    }
    __syncthreads();
    if(ROLE<4){  }
    if(ROLE==0){
      if(s<NSEG-1){
        f32x4 c={0,0,0,0},c2={0,0,0,0}; u32x2 ta[8];
        #pragma unroll
        for(int u=0;u<8;u++) ta[u]=trread(trp+(u>>1)*512+(u&1)*64);
        __builtin_amdgcn_sched_barrier(0);
        #pragma unroll
        for(int u=0;u<4;u++){
          u32x2 a0=ta[2*u], a1=ta[2*u+1];
          u32x4 f0={mb[4*u].x,mb[4*u].y,mb[4*u+1].x,mb[4*u+1].y};
          u32x4 f1= u<3? u32x4{mb[(4*u+2)%14].x,mb[(4*u+2)%14].y,mb[(4*u+3)%14].x,mb[(4*u+3)%14].y} : u32x4{0,0,0,0};
          if(u&1) c2=smf64(u32x4{a0.x,a0.y,a1.x,a1.y},cat(f0,f1),c2,idx); else c=smf64(u32x4{a0.x,a0.y,a1.x,a1.y},cat(f0,f1),c,idx);
        }
        __builtin_amdgcn_sched_barrier(0);
        c+=c2; float sa=swadd(c[0],c[2])*CSC, sb=swadd(c[1],c[3])*CSC;
        if(r<8) ST64((u32x2{canon(BC(u32,sa)),canon(BC(u32,sb))}),hot,base+32768+(((h*J+j)*256+(g>>1)*128+r*16+(g&1)*8)^X),16);
      }

      dl=now()+P.dA;
    } else if(ROLE==2){ dl=now()+P.dD;
    } else if(ROLE==1){
      dl=now(); if(s<NSEG-1) until(dl+P.dE); dl+=P.dD;
      f32x4 c={0,0,0,0};
      #pragma unroll
      for(int u=0;u<4;u++){
        u32x2 a0=trread(trp+u*512), a1=trread(trp+u*512+64);
        c=smf64(u32x4{a0.x,a0.y,a1.x,a1.y},cat(xb[2*u],u<3?xb[(2*u+1)%7]:u32x4{0,0,0,0}),c,idx);
      }
      float sa=swadd(c[0],c[2]), sb=swadd(c[1],c[3]);
      ST64((u32x2{canon(BC(u32,sa)),canon(BC(u32,sb))}),sxm,((s*H+h)*J+j)*1024+(w-1)*512+(g&1)*256+r*16+(g>>1)*8,16);
      const u32 xn=xo+min(s+1,NSEG-1)*32*D*2;
      #pragma unroll
      for(int k=0;k<7;k++) xb[k]=LD128(xrs,xn+64*k,0);
    } else if(ROLE==3 && s<NSEG-1){
      float sm=(v0+v1)+(v2+v3);
      sm+=dpp<0xB1>(sm); sm+=dpp<0x4E>(sm); sm+=dpp<0x141>(sm);
      float rl=__builtin_amdgcn_rcpf(sm);
      ST64((u32x2{canon(pk(v0*rl,v1*rl)),canon(pk(v2*rl,v3*rl))}),hot,base+((16384+h*512+l*8)^X),16);
      if((l&7)==0) ST32(canon(BC(u32,mc+__builtin_amdgcn_logf(sm))),hot,base+((7168+h*32+(l>>3)*4)^X),16);

      until(now()+P.dE);
      if(have<s+2){ int v=waitprog(flg,2*h+(l&1),s+2,dead,1); have=min(__builtin_amdgcn_readlane(v,0),__builtin_amdgcn_readlane(v,1)); }
      const u32 o=(u32)((s+1)*H+h)*14*2048+l*16;
      #pragma unroll
      for(int k=0;k<14;k++) W[k/7][k%7]=cat(LD128(kq,o+k*2048,16),LD128(kq,o+k*2048+1024,16));
    }
  }

}

template<int FIN>
DEV void value_loop(const ChainArgs& P,char* lds,int c,int w,int l,int cls){
  const int r=l&15,g=l>>4; char* ws=P.ws;
  auto hot=mk(ws+O_HOT,66*65536);
  const u32 XX=loadX(ws,cls);
  const int idx=(r<8)?0x4444:0xEEEE;
  int dead=SPIN_MAX;
  const int pr=FIN?2*w:2*(w-4)+1;
  u32x8 W[2][7];
  #pragma unroll
  for(int hh=0;hh<2;hh++){ const char* p=ws+O_BT+wf(2*pr+hh,16*c+r,g);
    #pragma unroll
    for(int t=0;t<7;t++) W[hh][t]=cat(*(const u32x4*)(p+4096*t),*(const u32x4*)(p+4096*t+2048)); }
  auto flg=mk(ws+O_FLG,16384); int have=0;
  auto vor=mk(ws+O_VO,NSEG*NV*8*2048);
  auto upr=mk(ws+O_UP,NSEG*NV*8*512);
  u32 voo=(c*8+pr)*2048+l*16;
  u32x8 vo0,vo1;
  if(FIN) __builtin_amdgcn_s_setprio(2);
  const int i=l&7, hi=(l>>3)&1;
  const u32 dm0=(i==2*hi?0xFFFFu:0u)|(i==2*hi+1?0xFFFF0000u:0u), dm1=(i==4+2*hi?0xFFFFu:0u)|(i==5+2*hi?0xFFFF0000u:0u);
  const u32 oEX=16384+(4*w+hi)*512+i*64+g*16, oSM=(4*w+g)*J*256+hi*128+i*16, oL=7168+(4*w+g)*32+i*4;
  float* redw=(float*)(lds+L_RED)+w*144+((r>>1)&1)*72+(4*(g&1)+2*(l>>5))*8+(r>>2)*2+(r&1);
  const char* redr=lds+L_RED+((l>>5)*72+(l&31)*2)*4;
  char* ux=lds+L_U+(w&3)*512+l*8;
  __builtin_amdgcn_s_waitcnt(0x0F70);

  u32 dl=now();
  for(int s=0;s<NSEG;s++){
    const u32 X=XM(s), X1=XM(s+1); const u32 base=s*65536u;
    if(!FIN){ until(dl); const u32 mb=s?base:64*65536u, mx=s?X:0u; const u32 o0=mb+(((w-4)*2048+l*16)^mx), o1=mb+((min((w-4)*2048+1024,6144)+l*16)^mx); u32x4 v0,v1;
      #pragma unroll 1
      do{ asm volatile("":::"memory"); v0=LD128(hot,o0,16); v1=LD128(hot,o1,16);
      } while(anysent(max(umax3(umax3(v0.x,v0.y,v0.z),v0.w,v1.x),umax3(v1.y,v1.z,v1.w))) && --dead>0);

      *(u32x4*)(lds+L_MEM+(w-4)*2048+l*16)=v0; if(w<7) *(u32x4*)(lds+L_MEM+(w-4)*2048+1024+l*16)=v1; }
    vmwait();
    __syncthreads();
    if(w==4 && l==0) ST32((u32)s,mk(ws+O_FLG,32768),(192+c)*128,16);

    if(FIN){ dl=now()+P.dB;
      if(s<NSEG-1){ if(have<s+1) have=__builtin_amdgcn_readfirstlane(waitprog(flg,32+c,s+1,dead,1));
        vo0=cat(LD128(vor,voo,16),LD128(vor,voo+1024,16)); vo1=cat(LD128(vor,voo+2048,16),LD128(vor,voo+3072,16)); voo+=NV*8*2048; } }
    f32x4 c0={0,0,0,0},c1={0,0,0,0}; u32x4 am[7];
    #pragma unroll
    for(int t=0;t<7;t++) am[t]=*(const u32x4*)(lds+L_MEM+t*1024+l*16);
    __builtin_amdgcn_sched_barrier(0);
    #pragma unroll
    for(int t=0;t<7;t++){ c0=smf64(am[t],W[0][t],c0,idx); c1=smf64(am[t],W[1][t],c1,idx); }
    u32x2 u={canon(pk(swadd(c0[0],c1[0]),swadd(c0[1],c1[1]))), canon(pk(swadd(c0[2],c1[2]),swadd(c0[3],c1[3])))};
    ST64(u,upr,((s*NV+c)*8+pr)*512+l*8,16);
    if(s==NSEG-1) break;
    if(!FIN) *(u32x2*)ux=u;
    __syncthreads();

    if(FIN){
      u32x2 u2=*(const u32x2*)ux;
      u32x4 e0,e1,p0,p1;
      until(dl-P.dX);
      u32 Lx=LD32(hot,base+(oL^X),16); e0=LD128(hot,base+(oEX^X),16); e1=LD128(hot,base+((oEX+1024)^X),16);
      until(dl);
      p0=LD128(hot,base+32768+(oSM^X),16); p1=LD128(hot,base+32768+((oSM+256)^X),16);
      if(anysent(max(Lx,max8(e0,e1)))){
        #pragma unroll 1
        do{ asm volatile("":::"memory"); Lx=LD32(hot,base+(oL^X),16); e0=LD128(hot,base+(oEX^X),16); e1=LD128(hot,base+((oEX+1024)^X),16); } while(anysent(max(Lx,max8(e0,e1))) && --dead>0); --dead; }
      f32x4 cx0=smf64(e0,vo0,f32x4{0,0,0,0},idx), cx1=smf64(e1,vo1,f32x4{0,0,0,0},idx);
      if(anysent(max8(p0,p1))){
        #pragma unroll 1
        do{ asm volatile("":::"memory"); p0=LD128(hot,base+32768+(oSM^X),16); p1=LD128(hot,base+32768+((oSM+256)^X),16); } while(anysent(max8(p0,p1)) && --dead>0); --dead; }

      f32x4 t=BC(f32x4,p0)+(BC(f32x4,p1)-BC(float,Lx));
      float m=vmax3(vmax3(t[0],t[1],t[2]),t[3],0.f); m=maxdpp<0x128>(m);
      float a0=__builtin_amdgcn_exp2f(t[0]-m),a1=__builtin_amdgcn_exp2f(t[1]-m),a2=__builtin_amdgcn_exp2f(t[2]-m),a3=__builtin_amdgcn_exp2f(t[3]-m),f=__builtin_amdgcn_exp2f(-m);
      float sm=(a0+a1)+(a2+a3); sm+=dpp<0x128>(sm);
      float rl=__builtin_amdgcn_rcpf(sm+f), fl=f*rl; u32 ff=pk(fl,fl);
      u32x8 bz={u.x,u.y,pk(cx0[0],cx0[1]),pk(cx0[2],cx0[3]),u2.x,u2.y,pk(cx1[0],cx1[1]),pk(cx1[2],cx1[3])};
      f32x4 o=smf64(u32x4{pk(a0*rl,a1*rl),ff&dm0,pk(a2*rl,a3*rl),ff&dm1},bz,f32x4{0,0,0,0},idx);
      redw[0]=swadd(o[0],o[2]); redw[8]=swadd(o[1],o[3]);
    }
    __syncthreads();
    if(w==7){
      f32x2 a=(*(const f32x2*)redr+*(const f32x2*)(redr+576))+(*(const f32x2*)(redr+2*576)+*(const f32x2*)(redr+3*576));
      ST32(canon(pk(a[0],a[1])),hot,base+65536+((c*256+l*4)^X1),16);
    }

    if(!FIN) dl=now()+(w==7?P.dC-150:P.dC);
  }

}

#define ST128(v,r,o,aux) __builtin_amdgcn_raw_buffer_store_b128(v,r,o,0,aux)
constexpr int XROW=912, XBUF=32*XROW;
struct XStage{ u32x4 v[4]; int la[4]; };
DEV void xs_init(XStage& X,int tid){
  #pragma unroll
  for(int i=0;i<4;i++){ int p=min(tid+512*i,1791); X.la[i]=(p/56)*XROW+(p%56)*16; } }
DEV void xs_load(XStage& X,__amdgpu_buffer_rsrc_t xrs,int s,int tid){
  #pragma unroll
  for(int i=0;i<4;i++) X.v[i]=LD128(xrs,s*28672+min(tid+512*i,1791)*16,0); }
DEV void xs_put(const XStage& X,char* buf,int tid){
  #pragma unroll
  for(int i=0;i<4;i++) if(i<3||tid<256) *(u32x4*)(buf+X.la[i])=X.v[i]; }
DEV u32x4 pk8(const f32x16& a,int o){ return u32x4{pk(a[o],a[o+1]),pk(a[o+2],a[o+3]),pk(a[o+4],a[o+5]),pk(a[o+6],a[o+7])}; }

template<int SX>
DEV void kq_worker(const ChainArgs& P,char* lds,int slot,int w,int l,int tid){
  char* ws=P.ws; const int h=slot>>1,j=slot&1,m=l&31,hh=l>>5;
  auto xrs=mk(P.x,S*D*2); auto kq=mk(ws+O_KQ,NSEG*H*14*2048); auto sxx=mk(ws+O_SXX,NSEG*H*J*4096); auto flg=mk(ws+O_FLG,16384);
  XStage X; xs_init(X,tid); xs_load(X,xrs,0,tid);
  u32x4 W[28];
  if(!SX){ const char* p=ws+O_A+wf(h,224*j+32*w+sw23(m),hh);
    #pragma unroll
    for(int t=0;t<28;t++) W[t]=*(const u32x4*)(p+1024*t); }
  xs_put(X,lds,tid); xs_load(X,xrs,1,tid);
  __syncthreads();
  char* kl=lds+3*XBUF;
  int b0=0,b1=XBUF,b2=2*XBUF;
  for(int s=0;s<=NSEG;s++){
    if(s||SX){ xs_put(X,lds+b1,tid); xs_load(X,xrs,min(s+2,NSEG-1),tid); }
    __builtin_amdgcn_sched_barrier(0);
    f32x16 c;
    #pragma unroll
    for(int i=0;i<16;i++) c[i]=0.f;
    if(!SX){ if(s<NSEG){
      const char* xp=lds+b0+m*XROW+hh*16;
      u32x4 f[6];
      #pragma unroll
      for(int t=0;t<6;t++) f[t]=*(const u32x4*)(xp+32*t);
      #pragma unroll
      for(int t=0;t<28;t++){ __builtin_amdgcn_sched_barrier(0); c=mf32(W[t],f[t%6],c); if(t+6<28) f[t%6]=*(const u32x4*)(xp+32*(t+6)); }
      __builtin_amdgcn_sched_barrier(0);
      u32x4 k0=pk8(c,0),k1=pk8(c,8);
      if(s>=3) vmwait();
      const u32 o=(((s*H+h)*2+(m>>4))*14+7*j+w)*1024+((m&15)+16*hh)*16;
      ST128(k0,kq,o,16); ST128(k1,kq,o+512,16);
      char* q=kl+(s&1)*14336+w*2048+l*16; *(u32x4*)q=k0; *(u32x4*)(q+1024)=k1;
      if(!s) xs_put(X,lds+b1,tid); } }
    else if(s>0){
      const char* xp=lds+b2+m*XROW+448*j+hh*16; const char* q=kl+((s-1)&1)*14336+l*16;
      u32x4 f[14],e[14];
      #pragma unroll
      for(int t=0;t<14;t++){ e[t]=*(const u32x4*)(q+1024*t); f[t]=*(const u32x4*)(xp+32*t); }
      __builtin_amdgcn_sched_barrier(0);
      #pragma unroll
      for(int t=0;t<14;t++) c=mf32(e[t],f[t],c);
      vmwait();
      const u32 o=(((s-1)*H+h)*2+j)*4096+hh*512+m*16;
      #pragma unroll
      for(int q4=0;q4<4;q4++) ST128(BC(u32x4,(f32x4{c[4*q4],c[4*q4+1],c[4*q4+2],c[4*q4+3]})),sxx,o+q4*1024,16);
    }
    if(s<3||s==NSEG) vmwait();
    __syncthreads();
    if(tid==0) ST32((u32)((s<3||s==NSEG)?s+1:s),flg,slot*128,16);
    if(s==0&&!SX){ xs_load(X,xrs,2,tid);  }
    int t=b2; b2=b0; b0=b1; b1=t;
  }
}

DEV void vo_worker(const ChainArgs& P,char* lds,int c,int w,int l,int tid){
  char* ws=P.ws; const int r=l&15,g=l>>4;
  auto xrs=mk(P.x,S*D*2); auto vor=mk(ws+O_VO,NSEG*NV*8*2048); auto flg=mk(ws+O_FLG,16384);
  XStage X; xs_init(X,tid); xs_load(X,xrs,0,tid);
  u32x4 W[2][14];
  #pragma unroll
  for(int hh=0;hh<2;hh++){ const char* p=ws+O_BT+wf(2*w+hh,16*c+r,g);
    #pragma unroll
    for(int t=0;t<14;t++) W[hh][t]=*(const u32x4*)(p+2048*t); }
  xs_put(X,lds,tid); xs_load(X,xrs,1,tid);
  __syncthreads();
  for(int s=0;s<NSEG;s++){
    xs_put(X,lds+((s+1)&1)*XBUF,tid); xs_load(X,xrs,min(s+2,NSEG-1),tid);
    __builtin_amdgcn_sched_barrier(0);
    f32x4 a[2][2]={{{0,0,0,0},{0,0,0,0}},{{0,0,0,0},{0,0,0,0}}};
    const char* xp=lds+(s&1)*XBUF+r*XROW+g*16;
    u32x4 f[6];
    #pragma unroll
    for(int t=0;t<6;t++) f[t]=*(const u32x4*)(xp+(t&1)*16*XROW+64*(t>>1));
    #pragma unroll
    for(int t=0;t<28;t++){ __builtin_amdgcn_sched_barrier(0); a[t&1][0]=mf16(f[t%6],W[0][t>>1],a[t&1][0]); a[t&1][1]=mf16(f[t%6],W[1][t>>1],a[t&1][1]);
      if(t+6<28) f[t%6]=*(const u32x4*)(xp+(t&1)*16*XROW+64*((t+6)>>1)); }
    __builtin_amdgcn_sched_barrier(0);
    if(s>=3) vmwait();
    #pragma unroll
    for(int hf=0;hf<2;hf++) ST128((u32x4{pk(a[hf][0][0],a[hf][0][1]),pk(a[hf][1][0],a[hf][1][1]),pk(a[hf][0][2],a[hf][0][3]),pk(a[hf][1][2],a[hf][1][3])}),vor,(((s*NV+c)*8+w)*2+hf)*1024+l*16,16);
    if(s<3||s==NSEG-1) vmwait();
    __syncthreads();
    if(tid==0) ST32((u32)((s<3||s==NSEG-1)?s+1:s),flg,(32+c)*128,16);
  }
}

DEV float sum16(float v){ v+=dpp<0xB1>(v); v+=dpp<0x4E>(v); v+=dpp<0x141>(v); v+=dpp<0x140>(v); return v; }
DEV float gelu(float a){ float u=a*(1.5957691216057308f+0.07135481282f*a*a); return a*__builtin_amdgcn_rcpf(1.f+__builtin_amdgcn_exp2f(-1.4426950408889634f*u)); }
DEV void mlp_worker(const ChainArgs& P,char* lds,int s,int w,int l,int tid){
  char* ws=P.ws; const int m=l&31,hh=l>>5;
  char* HL=lds+XBUF+256; float* GL=(float*)(lds+XBUF);
  { auto wr=mk(ws+O_W1,2*D*F*2);
    #pragma unroll 1
    for(int k0=tid;k0<3136;k0+=512){ const int id=s*3136+k0; const bool two=id>=100352; const int k=two?id-100352:id, ln=k&63, mm=ln&31, h2=ln>>5, fr=k>>6;
      const u16* p; int str;
      if(!two){ p=P.w1+(size_t)(16*(fr%28)+8*h2)*F+32*(fr/28)+sw23(mm); str=F; } else { p=P.w2+(size_t)(16*(fr%112)+8*h2)*D+32*(fr/112)+mm; str=D; }
      u32 e[8];
      #pragma unroll
      for(int j=0;j<8;j++) e[j]=p[j*str];
      ST128((u32x4{e[0]|(e[1]<<16),e[2]|(e[3]<<16),e[4]|(e[5]<<16),e[6]|(e[7]<<16)}),wr,id*16,16); }
    vmwait(); __syncthreads();
    if(tid==0) ST32(1u,mk(ws+O_FLG,32768),(128+s)*128,16); }
  { const int row=4*w+(l>>4), q=l&15; const u16* xr=P.x+(size_t)(s*32+row)*D;
    float v[4][8]; float sm=0,gd=0;
    #pragma unroll
    for(int i=0;i<4;i++){ const bool ok=(i<3||q<8); const int ch=ok?q+16*i:0; u32x4 a=*(const u32x4*)(xr+ch*8), b=*(const u32x4*)(P.gw+ch*8);
      #pragma unroll
      for(int k=0;k<4;k++){ u32 av=ok?a[k]:0u; u32 bv=b[k]; v[i][2*k]=lo16(av); v[i][2*k+1]=hi16(av); sm+=v[i][2*k]+v[i][2*k+1]; gd+=v[i][2*k]*lo16(bv)+v[i][2*k+1]*hi16(bv); } }
    sm=sum16(sm); gd=sum16(gd); const float mean=sm*(1.f/D); float vr=0;
    #pragma unroll
    for(int i=0;i<4;i++){ const bool ok=(i<3||q<8);
      #pragma unroll
      for(int k=0;k<8;k++){ v[i][k]-=mean; vr+=ok?v[i][k]*v[i][k]:0.f; } }
    vr=sum16(vr); const float rs=1.f/sqrtf(vr*(1.f/D)+1e-5f);
    #pragma unroll
    for(int i=0;i<4;i++) if(i<3||q<8) *(u32x4*)(lds+row*XROW+(q+16*i)*16)=u32x4{pk(v[i][0]*rs,v[i][1]*rs),pk(v[i][2]*rs,v[i][3]*rs),pk(v[i][4]*rs,v[i][5]*rs),pk(v[i][6]*rs,v[i][7]*rs)};
    const float gt=1.f/(1.f+__expf(-gd));
    if(q==0){ GL[row]=1.f-gt; ST32(BC(u32,gt),mk(ws+O_G,S*4),(s*32+row)*4,16); }
  }
  { int dead=SPIN_MAX; waitprog(mk(ws+O_FLG,32768),128+l,1,dead,8); __builtin_amdgcn_fence(__ATOMIC_ACQUIRE,"agent"); }
  __syncthreads();
  { const char* yp=lds+m*XROW+hh*16;
    #pragma unroll 1
    for(int ft=w;ft<56;ft+=8){
      const char* p=ws+O_W1+(size_t)ft*28672+l*16; u32x4 Wt[28];
      #pragma unroll
      for(int t=0;t<28;t++) Wt[t]=*(const u32x4*)(p+1024*t);
      u32x4 ba=*(const u32x4*)(P.b1+32*ft+8*hh), bb=*(const u32x4*)(P.b1+32*ft+16+8*hh);
      f32x16 c;
      #pragma unroll
      for(int k=0;k<4;k++){ u32 x0=ba[k],x1=bb[k]; c[2*k]=lo16(x0); c[2*k+1]=hi16(x0); c[8+2*k]=lo16(x1); c[9+2*k]=hi16(x1); }
      #pragma unroll
      for(int t=0;t<28;t++) c=mf32(Wt[t],*(const u32x4*)(yp+32*t),c);
      #pragma unroll
      for(int k=0;k<16;k++) c[k]=gelu(c[k]);
      *(u32x4*)(HL+ft*2048+l*16)=pk8(c,0); *(u32x4*)(HL+ft*2048+1024+l*16)=pk8(c,8);
    } }
  __syncthreads();
  if(w<7){
    f32x16 c[2];
    #pragma unroll
    for(int i=0;i<2;i++){
      #pragma unroll
      for(int q=0;q<4;q++){ u32x2 b=*(const u32x2*)(P.b2+64*w+32*i+8*q+4*hh); u32 x0=b.x,x1=b.y; c[i][4*q]=lo16(x0); c[i][4*q+1]=hi16(x0); c[i][4*q+2]=lo16(x1); c[i][4*q+3]=hi16(x1); } }
    #pragma unroll 1
    for(int kb=0;kb<8;kb++){
      const char* p=ws+O_W2+(size_t)(2*w*112+kb*14)*1024+l*16; u32x4 Wt[2][14];
      #pragma unroll
      for(int t=0;t<14;t++){ Wt[0][t]=*(const u32x4*)(p+1024*t); Wt[1][t]=*(const u32x4*)(p+112*1024+1024*t); }
      #pragma unroll
      for(int t=0;t<14;t++){ u32x4 hb=*(const u32x4*)(HL+(kb*14+t)*1024+l*16); c[0]=mf32(Wt[0][t],hb,c[0]); c[1]=mf32(Wt[1][t],hb,c[1]); }
    }
    const float og=GL[m]; auto bbr=mk(ws+O_BB,S*D*4);
    #pragma unroll
    for(int i=0;i<2;i++){
      #pragma unroll
      for(int q=0;q<4;q++) ST128(BC(u32x4,(f32x4{c[i][4*q]*og,c[i][4*q+1]*og,c[i][4*q+2]*og,c[i][4*q+3]*og})),bbr,((s*112+16*w+8*i+2*q+hh)*32+m)*16,16); }
  }
  vmwait(); __syncthreads();
  if(tid==0) ST32(1u,mk(ws+O_FLG,16384),(64+s)*128,16);
}

DEV float max16x(float v){ auto r=__builtin_amdgcn_permlane16_swap(BC(u32,v),BC(u32,v),false,false); u32 a=r[0],b=r[1]; return vmax3(BC(float,a),BC(float,b),BC(float,b)); }
DEV float add16x(float v){ auto r=__builtin_amdgcn_permlane16_swap(BC(u32,v),BC(u32,v),false,false); u32 a=r[0],b=r[1]; return BC(float,a)+BC(float,b); }
DEV float max32x(float v){ auto r=__builtin_amdgcn_permlane32_swap(BC(u32,v),BC(u32,v),false,false); u32 a=r[0],b=r[1]; return vmax3(BC(float,a),BC(float,b),BC(float,b)); }
DEV void out_worker(const ChainArgs& P,char* lds,int sg,int cg,int w,int l,int tid){
  char* ws=P.ws; const int r=l&15,g=l>>4; int dead=SPIN_MAX;
  auto vor=mk(ws+O_VO,NSEG*NV*8*2048); auto flg=mk(ws+O_FLG,16384); auto sxx=mk(ws+O_SXX,NSEG*H*J*4096);
  auto sxm=mk(ws+O_SXM,NSEG*H*J*1024); auto upr=mk(ws+O_UP,NSEG*NV*8*512); auto bbr=mk(ws+O_BB,S*D*4); auto gr=mk(ws+O_G,S*4); auto outr=mk(P.out,S*D*2);
  #pragma unroll 1
  for(int s=sg;s<NSEG;s+=8){
    waitprog(flg,l<4?4*w+l:(l<8?32+4*cg+(l-4):64+s),l<4?s+2:(l<8?s+1:1),dead,16);
    f32x4 sv[2][2][2]; u32x4 vv[4][2];
    #pragma unroll
    for(int hh=0;hh<2;hh++){ const u32 o=((s*H+2*w+hh)*2)*4096+g*512+r*16;
      sv[hh][0][0]=BC(f32x4,LD128(sxx,o,16))+BC(f32x4,LD128(sxx,o+4096,16));
      sv[hh][1][0]=BC(f32x4,LD128(sxx,o+256,16))+BC(f32x4,LD128(sxx,o+4096+256,16));
      sv[hh][1][1]=BC(f32x4,LD128(sxx,o+2048+256,16))+BC(f32x4,LD128(sxx,o+4096+2048+256,16)); }
    #pragma unroll
    for(int ct=0;ct<4;ct++){ const u32 o=((s*NV+4*cg+ct)*8+w)*2048+l*16; vv[ct][0]=LD128(vor,o,16); vv[ct][1]=LD128(vor,o+1024,16); }
    const int ort=w>>2, oct=w&3;
    const f32x4 bb=BC(f32x4,LD128(bbr,((s*112+16*cg+4*oct+g)*32+16*ort+r)*16,16));
    const float gt=BC(float,__builtin_amdgcn_raw_buffer_load_b32(gr,(s*32+16*ort+r)*4,0,16));
    f32x4 tmp[2][2][4]; float mx[2],lx[2];
    #pragma unroll
    for(int hh=0;hh<2;hh++){
      #pragma unroll
      for(int rt=0;rt<2;rt++){
        f32x4 a=sv[hh][rt][0], b=sv[hh][rt][rt];
        #pragma unroll
        for(int i=0;i<4;i++) b[i]=(4*g+i<=r)?b[i]:-3.0e38f;
        float mm=vmax3(vmax3(b[0],b[1],b[2]),b[3],b[3]); if(rt) mm=vmax3(vmax3(a[0],a[1],mm),a[2],a[3]);
        mm=max32x(max16x(mm)); const float mc=mm*CSC; float sum=0;
        #pragma unroll
        for(int i=0;i<4;i++){ b[i]=(4*g+i<=r)?__builtin_amdgcn_exp2f(b[i]*CSC-mc):0.f; sum+=b[i]; if(rt){ a[i]=__builtin_amdgcn_exp2f(a[i]*CSC-mc); sum+=a[i]; } }
        sum=add16x(sum); sum=swadd(sum,sum);
        if(hh==0||g>=2){ mx[rt]=mc; lx[rt]=sum; }
        const u32x4 e= rt? u32x4{pk(a[0],a[1]),pk(a[2],a[3]),pk(b[0],b[1]),pk(b[2],b[3])} : u32x4{pk(b[0],b[1]),pk(b[2],b[3]),0u,0u};
        #pragma unroll
        for(int ct=0;ct<4;ct++) tmp[hh][rt][ct]=mf16(u32x4{vv[ct][0][hh],vv[ct][0][2+hh],vv[ct][1][hh],vv[ct][1][2+hh]},e,f32x4{0,0,0,0});
      } }
    const u32 om=((s*H+2*w+(g>>1))*2)*1024+(g&1)*256+r*16, ou=((s*NV+4*cg)*8+w)*512+l*8;
    { u32 v;
      #pragma unroll 1
      while(true){ asm volatile("":::"memory"); v=__builtin_amdgcn_raw_buffer_load_b32(sxm,((s*H+2*w)*2)*1024,0,16); if(__builtin_amdgcn_readfirstlane(v)!=SENT || --dead<=0) break; nap(4); } }
    if(s<NSEG-2) waitprog(mk(ws+O_FLG,32768),192+4*cg+(l&3),s+1,dead,8);
    u32x4 q[2][2]; u32x2 uu[4];
    #pragma unroll 1
    while(true){ asm volatile("":::"memory"); u32 mxs=0;
      #pragma unroll
      for(int rt=0;rt<2;rt++){ q[rt][0]=LD128(sxm,om+rt*512,16); q[rt][1]=LD128(sxm,om+1024+rt*512,16); }
      #pragma unroll
      for(int ct=0;ct<4;ct++) uu[ct]=LD64(upr,ou+ct*4096,16);
      #pragma unroll
      for(int rt=0;rt<2;rt++){ mxs=umax3(umax3(mxs,q[rt][0].x,q[rt][0].y),q[rt][0].z,q[rt][0].w); mxs=umax3(umax3(mxs,q[rt][1].x,q[rt][1].y),q[rt][1].z,q[rt][1].w); }
      #pragma unroll
      for(int ct=0;ct<4;ct++) mxs=umax3(mxs,uu[ct].x,uu[ct].y);
      if(!anysent(mxs) || --dead<=0) break; nap(2); }
    #pragma unroll
    for(int rt=0;rt<2;rt++){
      f32x4 t=(BC(f32x4,q[rt][0])+BC(f32x4,q[rt][1]))*CSC;
      float mm=max16x(vmax3(vmax3(t[0],t[1],t[2]),t[3],mx[rt]));
      float a0=__builtin_amdgcn_exp2f(t[0]-mm),a1=__builtin_amdgcn_exp2f(t[1]-mm),a2=__builtin_amdgcn_exp2f(t[2]-mm),a3=__builtin_amdgcn_exp2f(t[3]-mm),f=__builtin_amdgcn_exp2f(mx[rt]-mm);
      const float rl=__builtin_amdgcn_rcpf(add16x((a0+a1)+(a2+a3))+f*lx[rt]), sc=f*rl;
      auto sp=__builtin_amdgcn_permlane32_swap(BC(u32,sc),BC(u32,sc),false,false); u32 s0=sp[0],s1=sp[1]; const float sA=BC(float,s0),sB=BC(float,s1);
      const u32x2 pm={pk(a0*rl,a1*rl),pk(a2*rl,a3*rl)};
      #pragma unroll
      for(int ct=0;ct<4;ct++) *(f32x4*)(lds+((w*8+rt*4+ct)*64+l)*16)=mf16k(uu[ct],pm,tmp[0][rt][ct]*sA+tmp[1][rt][ct]*sB);
    }
    __syncthreads();
    f32x4 o=*(const f32x4*)(lds+(w*64+l)*16);
    #pragma unroll
    for(int k=1;k<8;k++) o+=*(const f32x4*)(lds+((k*8+w)*64+l)*16);
    o=o*gt+bb;
    ST64((u32x2{pk(o[0],o[1]),pk(o[2],o[3])}),outr,((s*32+16*ort+r)*D+64*cg+16*oct+4*g)*2,OAUX);
    __syncthreads();
  }
}

#define XS 3
#define MAP 4
constexpr int XV=XS^2, XK=XS^1, XO=XS^3;
constexpr int LDS2=XBUF+256+114688;
__global__ __launch_bounds__(512,2) void k_main(ChainArgs P){
  extern __shared__ __attribute__((aligned(16))) char lds[];
  const int rot=*(const int*)(P.ws+O_XOR)&7;
  const int b=blockIdx.x, xcd=(b+rot)&7, slot=b>>3, cls=((XS&4)-rot)&7;
  const int tid=threadIdx.x, l=tid&63, w=__builtin_amdgcn_readfirstlane(tid>>6);

  const int q=xcd&3, role=((xcd^XS)&4)?4:slot>>3, sl=q*8+(slot&7), c=(slot&7)<7?q*7+(slot&7):-1;
  if(role==1||role==3) until(now()+P.dG);
  if(role==0){
    if(w==0) score_loop<0>(P,lds,sl,w,l,cls);
    else if(w<3) score_loop<1>(P,lds,sl,w,l,cls);
    else if(w<7) score_loop<2>(P,lds,sl,w,l,cls);
    else if((sl&1)==0) score_loop<3>(P,lds,sl,w,l,cls);
    else score_loop<4>(P,lds,sl,w,l,cls);
  }
  else if(role==1){ if(c>=0){ if(w<4) value_loop<1>(P,lds,c,w,l,cls); else value_loop<0>(P,lds,c,w,l,cls); } }
  else if(role==2){ if(w<7) kq_worker<0>(P,lds,sl,w,l,tid); else kq_worker<1>(P,lds,sl,w,l,tid); }
  else if(role==3){ if(c>=0) vo_worker(P,lds,c,w,l,tid); }
  else if(((xcd^XS)&6)==4) mlp_worker(P,lds,slot*2+(xcd&1),w,l,tid);
  else if(slot<28) out_worker(P,lds,(slot/7)*2+(xcd&1),slot%7,w,l,tid);

}

struct PreArgs{ const u16* mem0; const u16* Wq; const u16* Wk; const u16* Wv; const u16* Wo; const u16* w1; const u16* w2; char* ws; };
DEV void sentinels(char* ws,u32 i0){
  constexpr u32 U0=(NSEG-2)*NV*8*512, UN=(u32)(O_SXX-O_UP)-U0;
  const u32x4 sent={SENT,SENT,SENT,SENT}; auto hot=mk(ws+O_HOT,64*65536); auto up=mk(ws+O_UP+U0,UN);
  if(((i0>>9)&7)<6){
    #pragma unroll
    for(u32 k=0;k<8;k++) ST128(sent,hot,(i0+k*32768)*16,16); }
  #pragma unroll
  for(u32 k=0;k<(UN+524287)/524288;k++) if((i0+k*32768)*16<UN) ST128(sent,up,(i0+k*32768)*16,16);
}
template<int WH>
DEV void ab_gemm(const PreArgs& P,char* lds,int h,int mb,int w,int l,int tid){
  const int m=l&31,hh=l>>5; u32x4 st[4][8];
  const u16* Np=(WH?P.Wv:P.Wk)+(size_t)(tid>>3)*4096+h*256+(tid&7)*8;
  const u16* Mp= WH? P.Wo+(size_t)(h*256+(tid>>3))*D+64*mb+(tid&7)*8 : P.Wq+(size_t)(64*mb+(tid>>3))*4096+h*256+(tid&7)*8;
  #pragma unroll
  for(int q=0;q<4;q++){
    #pragma unroll
    for(int i=0;i<7;i++) st[q][i]=*(const u32x4*)(Np+(size_t)i*64*4096+64*q);
    st[q][7]=*(const u32x4*)(Mp+(WH?64*D:64)*q); }
  const int la=(tid>>3)*128+(((tid&7)^((tid>>4)&7))*16), lam=WH?57344+tid*16:57344+la;
  f32x16 c[2][2];
  #pragma unroll
  for(int i=0;i<16;i++){ c[0][0][i]=0;c[0][1][i]=0;c[1][0][i]=0;c[1][1][i]=0; }
  const int ra=sw23(m);
  #pragma unroll
  for(int hk=0;hk<2;hk++){
    if(hk) __syncthreads();
    #pragma unroll
    for(int q=0;q<2;q++){
      #pragma unroll
      for(int i=0;i<7;i++) *(u32x4*)(lds+q*65536+i*8192+la)=st[2*hk+q][i];
      *(u32x4*)(lds+q*65536+lam)=st[2*hk+q][7]; }
    __syncthreads();
    if(w==7) sentinels(P.ws,blockIdx.x*128+hk*64+l);
    else {
      #pragma unroll
      for(int q=0;q<2;q++){
        #pragma unroll
        for(int t=0;t<4;t++){ u32x4 a[2],b[2];
          #pragma unroll
          for(int i=0;i<2;i++){
            a[i]=*(const u32x4*)(lds+q*65536+(64*w+32*i+ra)*128+(((2*t+hh)^((ra>>1)&7))*16));
            if(WH){ const char* p=lds+q*65536+57344+(16*t+8*hh+((l&15)>>2))*128+(32*i+16*((l>>4)&1)+4*(l&3))*2; u32x2 x0=trread(p),x1=trread(p+512); b[i]=u32x4{x0.x,x0.y,x1.x,x1.y}; }
            else b[i]=*(const u32x4*)(lds+q*65536+57344+(32*i+m)*128+(((2*t+hh)^((m>>1)&7))*16)); }
          c[0][0]=mf32(a[0],b[0],c[0][0]); c[0][1]=mf32(a[0],b[1],c[0][1]); c[1][0]=mf32(a[1],b[0],c[1][0]); c[1][1]=mf32(a[1],b[1],c[1][1]);
        } } } }
  if(w<7){ auto o=mk(P.ws+(WH?O_BT:O_A),H*D*D*2);
    #pragma unroll
    for(int ti=0;ti<2;ti++){
      #pragma unroll
      for(int tj=0;tj<2;tj++){ const u32 p=wf(h,64*mb+32*tj+m,8*w+4*ti+hh); ST128(pk8(c[ti][tj],0),o,p,KAUX); ST128(pk8(c[ti][tj],8),o,p+1024,KAUX); } } }
}
__global__ __launch_bounds__(512,2) void k_pre(PreArgs P){
  extern __shared__ __attribute__((aligned(16))) char lds[];
  const int b=blockIdx.x, xcd=b&7, slot=b>>3;
  const int tid=threadIdx.x, l=tid&63, w=__builtin_amdgcn_readfirstlane(tid>>6);
  char* ws=P.ws; const u32x4 sent={SENT,SENT,SENT,SENT}; auto hot=mk(ws+O_HOT,66*65536);

  if(slot<28){ const int gm=xcd*4+slot/7; if(gm&1) ab_gemm<1>(P,lds,gm>>1,slot%7,w,l,tid); else ab_gemm<0>(P,lds,gm>>1,slot%7,w,l,tid); }
  else {
    const int mi=(slot-28)*8+xcd;
    {
      u32* p=(u32*)(ws+O_HOT+(size_t)w*524288+32768+(slot-28)*8192+xcd*1024); u32 best=0xFFFFFu,v;
      asm volatile("global_store_dword %0, %1, off sc1\n s_waitcnt vmcnt(0)"::"v"(p+(l&7)*32),"v"(SENT):"memory");
      __syncthreads();
      const u32 tb=now();
      for(int i=0;i<CALN;i++){ until(tb+(i*8+w)*CALT); u32 t0=now(); asm volatile("global_load_dword %0, %1, off sc1\n s_waitcnt vmcnt(0)":"=v"(v):"v"(p+i*32):"memory"); best=min(best,now()-t0); }
      if(l==0) ((u32*)(ws+O_XOR))[16+xcd*32+(slot-28)*8+w]=best;
    }
    if(b==224 && tid==0){ u32 v; asm volatile("s_getreg_b32 %0, hwreg(HW_REG_XCC_ID)":"=s"(v)); *(u32*)(ws+O_XOR)=v&7; }
    if(mi==1){ for(int k=tid;k<1792;k+=512){ int c=k>>6, hi=(k>>5)&1, i=(k>>2)&7, q=k&3; u32 v=canon(*(const u32*)(P.mem0+i*D+16*c+4*q+2*hi)); ST32(v,hot,64*65536+k*4,16); }
      if(tid<224) ST32(0u,mk(ws+O_FLG,32768),tid*128,16); }
  }

  if(slot>=28 && tid<128) sentinels(ws,b*128+tid);

}
extern "C" void kernel_launch(void* const* d_in,const int* in_sizes,int n_in,void* d_out,int out_size,void* d_ws,size_t ws_size,hipStream_t stream){
  char* ws=(char*)(((size_t)d_ws+2097151)&~(size_t)2097151);
  (void)hipFuncSetAttribute((const void*)k_pre,hipFuncAttributeMaxDynamicSharedMemorySize,131072);
  (void)hipFuncSetAttribute((const void*)k_main,hipFuncAttributeMaxDynamicSharedMemorySize,LDS2);
  PreArgs Q{(const u16*)d_in[1],(const u16*)d_in[2],(const u16*)d_in[3],(const u16*)d_in[4],(const u16*)d_in[5],(const u16*)d_in[8],(const u16*)d_in[10],ws};
  k_pre<<<256,512,131072,stream>>>(Q);
  ChainArgs P{(const u16*)d_in[0],ws,(const u16*)d_in[9],(const u16*)d_in[11],(const u16*)d_in[12],(const u16*)d_in[8],(const u16*)d_in[10],(u16*)d_out,2050,1200,500,2350,100,0,2000,0};
  k_main<<<256,512,LDS2,stream>>>(P);
}
```
